```python
import torch

def _softmax(x, axis=-1):
    return torch.softmax(x, dim=axis)
def _gelu(x):
    return torch.nn.functional.gelu(x, approximate="tanh")
def _silu(x):
    return torch.nn.functional.silu(x)
def _sigmoid(x):
    return torch.sigmoid(x)
def _layernorm(x, axis=-1, eps=1e-5):
    m = x.mean(dim=axis, keepdim=True)
    v = x.var(dim=axis, keepdim=True, unbiased=False)
    return (x - m) / torch.sqrt(v + eps)
def _scatter_add(tgt, idx, src, axis=0):
    return tgt.clone().index_add_(axis, idx, src)
def _segment_sum(src, seg, n):
    out = torch.zeros((n,) + src.shape[1:], dtype=src.dtype)
    return out.index_add_(0, seg, src)
def _pad(x, pw):
    flat = tuple(v for pair in reversed(pw) for v in pair)
    return torch.nn.functional.pad(x, flat)

kernel_name = 'recurrent_scan__sliding_block__glu_gated_bilinear__prompt'

def setup_inputs():

    _w = (lambda a: (lambda t: t.to(torch.bfloat16) if t.is_floating_point() else t)(torch.as_tensor(a)))
    def _A_setup():
        B, S, D = 1, 2048, 448
        return {
            "x": _w((lambda *s: torch.randn(*s, dtype=torch.bfloat16) * 0.05)(B, S, D)),
            "wqkv": _w((lambda *s: torch.randn(*s, dtype=torch.bfloat16) * 0.2)(D, 3 * D)),
            "wo": _w((lambda *s: torch.randn(*s, dtype=torch.bfloat16) * 0.2)(D, D)),
        }
    _A_in = _A_setup()

    def _B_setup():
        B, S, D, F = 1, 2048, 448, 1792
        return {
            "x": _w((lambda *s: torch.randn(*s, dtype=torch.bfloat16) * 0.05)(B, S, D)),
            "w_gate": _w((lambda *s: torch.randn(*s, dtype=torch.bfloat16) * 0.1)(D, F) * (D ** -0.5)),
            "w_up": _w((lambda *s: torch.randn(*s, dtype=torch.bfloat16) * 0.1)(D, F) * (D ** -0.5)),
            "w_down": _w((lambda *s: torch.randn(*s, dtype=torch.bfloat16) * 0.1)(F, D) * (F ** -0.5)),
        }
    _B_in = _B_setup()

    return {
        "A_x": _A_in["x"],
        "A_wqkv": _A_in["wqkv"],
        "A_wo": _A_in["wo"],
        "B_x": _B_in["x"],
        "B_w_gate": _B_in["w_gate"],
        "B_w_up": _B_in["w_up"],
        "B_w_down": _B_in["w_down"],
    }

def reference(A_x, A_wqkv, A_wo, B_x, B_w_gate, B_w_up, B_w_down):
    def _A_ref(x, wqkv, wo):
        B, S, D = x.shape
        H = 16; Dh = D // H; W = 256
        qkv = (lambda x, *d: x.permute(*d))((x @ wqkv).reshape(B, S, 3, H, Dh), 2, 0, 3, 1, 4)
        q, k, v = qkv[0], qkv[1], qkv[2]
        scores = torch.einsum("bhqd,bhkd->bhqk", q, k) * (Dh ** -0.5)
        idx = torch.arange(S)
        mask = (idx[:, None] - idx[None, :] >= 0) & (idx[:, None] - idx[None, :] < W)
        scores = torch.where(mask[None, None], scores, -1e9)
        p = _softmax(scores, axis=-1)
        o = torch.einsum("bhqk,bhkd->bhqd", p, v)
        return (lambda x, *d: x.permute(*d))(o, 0, 2, 1, 3).reshape(B, S, D) @ wo

    def _B_ref(x, w_gate, w_up, w_down):
        g = torch.einsum("bsd,df->bsf", x, w_gate)
        u = torch.einsum("bsd,df->bsf", x, w_up)
        h = g * u
        return torch.einsum("bsf,fd->bsd", h, w_down)

    x = A_x
    xn = _layernorm(x, axis=-1)
    s = _A_ref(xn, A_wqkv, A_wo)
    g = _silu(_B_ref(xn, B_w_gate, B_w_up, B_w_down))
    return x + s * g
```

```cpp
#include <hip/hip_runtime.h>
#include <hip/hip_bf16.h>
typedef unsigned short u16;
__device__ __forceinline__ float bf2f(u16 v){ return __uint_as_float(((unsigned)v)<<16); }
__device__ __forceinline__ u16 f2bf(float f){ unsigned u=__float_as_uint(f); u += 0x7fffu + ((u>>16)&1u); return (u16)(u>>16); }
__device__ __forceinline__ float rbf(float f){ return bf2f(f2bf(f)); }

constexpr int S=2048, D=448, H=16, DH=28, W=256, F=1792;

__global__ void k_ln(const u16* x, u16* xn){
  int row=blockIdx.x; int t=threadIdx.x;
  float s=0; for(int i=t;i<D;i+=64) s+=bf2f(x[row*D+i]);
  for(int o=32;o>0;o>>=1) s+=__shfl_xor(s,o);
  float mean=s/D; float q=0;
  for(int i=t;i<D;i+=64){ float d=bf2f(x[row*D+i])-mean; q+=d*d; }
  for(int o=32;o>0;o>>=1) q+=__shfl_xor(q,o);
  float var=q/D;
  float mb=rbf(mean), vb=rbf(var);
  float den=rbf(sqrtf(rbf(vb+1e-5f)));
  for(int i=t;i<D;i+=64){ float d=rbf(bf2f(x[row*D+i])-mb); xn[row*D+i]=f2bf(d/den); }
}
__global__ void k_gemm(const u16* A,const u16* B,u16* C,int M,int N,int K){
  int n=blockIdx.x*64+threadIdx.x; int m=blockIdx.y;
  if(n>=N) return;
  float acc=0; for(int k=0;k<K;++k) acc+=bf2f(A[m*K+k])*bf2f(B[k*N+n]);
  C[m*N+n]=f2bf(acc);
}
__global__ void k_mul(const u16* g,const u16* u,u16* h,int n){ int i=blockIdx.x*256+threadIdx.x; if(i<n) h[i]=f2bf(bf2f(g[i])*bf2f(u[i])); }
__global__ void k_attn(const u16* qkv,u16* o){
  int i=blockIdx.x*64+threadIdx.x; int tok=i/H, h=i%H;
  float q[DH]; for(int d=0;d<DH;++d) q[d]=bf2f(qkv[tok*3*D+h*DH+d]);
  int lo=tok-W+1; if(lo<0) lo=0;
  float m=-1e30f;
  for(int j=lo;j<=tok;++j){ float s=0; for(int d=0;d<DH;++d) s+=q[d]*bf2f(qkv[j*3*D+D+h*DH+d]); s*=0.18898223650461362f; m=fmaxf(m,s);}
  float l=0; float acc[DH]; for(int d=0;d<DH;++d) acc[d]=0;
  for(int j=lo;j<=tok;++j){ float s=0; for(int d=0;d<DH;++d) s+=q[d]*bf2f(qkv[j*3*D+D+h*DH+d]); s*=0.18898223650461362f; float p=rbf(__expf(s-m)); l+=p; for(int d=0;d<DH;++d) acc[d]+=p*bf2f(qkv[j*3*D+2*D+h*DH+d]); }
  for(int d=0;d<DH;++d) o[tok*D+h*DH+d]=f2bf(acc[d]/l);
}
__global__ void k_final(const u16* x,const u16* s,const u16* f,u16* out,int n){ int i=blockIdx.x*256+threadIdx.x; if(i<n){ float a=bf2f(f[i]); float g=rbf(a/(1.f+__expf(-a))); out[i]=f2bf(bf2f(x[i])+rbf(bf2f(s[i])*g)); } }

extern "C" void kernel_launch(void* const* d_in, const int* in_sizes, int n_in, void* d_out, int out_size, void* d_ws, size_t ws_size, hipStream_t stream) {
  const u16* x=(const u16*)d_in[0]; const u16* wqkv=(const u16*)d_in[1]; const u16* wo=(const u16*)d_in[2];
  const u16* wg=(const u16*)d_in[4]; const u16* wu=(const u16*)d_in[5]; const u16* wd=(const u16*)d_in[6];
  u16* ws=(u16*)d_ws; u16* xn=ws; u16* qkv=xn+S*D; u16* g=qkv+S*3*D; u16* u=g+S*F; u16* h=u+S*F; u16* o=h+S*F; u16* s=o+S*D; u16* f=s+S*D;
  k_ln<<<S,64,0,stream>>>(x,xn);
  k_gemm<<<dim3(3*D/64,S),64,0,stream>>>(xn,wqkv,qkv,S,3*D,D);
  k_gemm<<<dim3(F/64,S),64,0,stream>>>(xn,wg,g,S,F,D);
  k_gemm<<<dim3(F/64,S),64,0,stream>>>(xn,wu,u,S,F,D);
  k_mul<<<S*F/256,256,0,stream>>>(g,u,h,S*F);
  k_attn<<<S*H/64,64,0,stream>>>(qkv,o);
  k_gemm<<<dim3(D/64,S),64,0,stream>>>(o,wo,s,S,D,D);
  k_gemm<<<dim3(D/64,S),64,0,stream>>>(h,wd,f,S,D,F);
  k_final<<<S*D/256,256,0,stream>>>(x,s,f,(u16*)d_out,S*D);
}
```

```cpp
#include <hip/hip_runtime.h>
#include <stdint.h>
#include <type_traits>

typedef unsigned short u16;
typedef unsigned long long u64;
typedef short bf16x8 __attribute__((ext_vector_type(8)));
typedef short s16x4 __attribute__((ext_vector_type(4)));
typedef float f32x4 __attribute__((ext_vector_type(4)));
typedef float f32x16 __attribute__((ext_vector_type(16)));
typedef float f32x2 __attribute__((ext_vector_type(2)));
typedef unsigned u32x4 __attribute__((ext_vector_type(4)));
typedef unsigned u32x2 __attribute__((ext_vector_type(2)));
typedef __bf16 bf2 __attribute__((ext_vector_type(2)));
#define LAS __attribute__((address_space(3)))
#define GAS __attribute__((address_space(1)))
#define DI __device__ __forceinline__

constexpr size_t WS_E = 0;
constexpr size_t WS_FS = 4096;
constexpr size_t WS_FQ = 8192;
constexpr size_t WS_FH = 32768;
constexpr size_t WS_FO = 98304;
constexpr size_t WS_Q = 131072;
constexpr size_t WS_K = WS_Q + 2097152;
constexpr size_t WS_V = WS_K + 2097152;
constexpr size_t WS_H = WS_V + 2097152;
constexpr size_t WS_O = WS_H + 7340032;
constexpr size_t WS_END = WS_O + 2097152;

template <int N> using ic = std::integral_constant<int, N>;
template <int B, int E, class Fn> DI void static_for(Fn&& f) { if constexpr (B < E) { f(ic<B>{}); static_for<B + 1, E>(f); } }

DI unsigned pk_bf16(float a, float b) { f32x2 v = {a, b}; bf2 r = __builtin_convertvector(v, bf2); return __builtin_bit_cast(unsigned, r); }
DI float lo_f(unsigned p) { return __uint_as_float(p << 16); }
DI float hi_f(unsigned p) { return __uint_as_float(p & 0xffff0000u); }
DI float rbf(float a) { return lo_f(pk_bf16(a, 0.f)); }
#define MFMA16(a, b, c) __builtin_amdgcn_mfma_f32_16x16x32_bf16((a), (b), (c), 0, 0, 0)
#define MFMA32(a, b, c) __builtin_amdgcn_mfma_f32_32x32x16_bf16((a), (b), (c), 0, 0, 0)
#define SBAR() __builtin_amdgcn_sched_barrier(0)
DI void glds16(const void* g, unsigned lds_base) {
    unsigned sv;
    asm volatile("s_mov_b32 %0, m0\n\ts_mov_b32 m0, %2\n\ts_nop 0\n\tglobal_load_lds_dwordx4 %1, off\n\ts_mov_b32 m0, %0" : "=&s"(sv) : "v"(g), "s"(lds_base) : "memory");
}
DI void glds16_sc1(const void* g, unsigned lds_base) {
    unsigned sv;
    asm volatile("s_mov_b32 %0, m0\n\ts_mov_b32 m0, %2\n\ts_nop 0\n\tglobal_load_lds_dwordx4 %1, off sc1\n\ts_mov_b32 m0, %0" : "=&s"(sv) : "v"(g), "s"(lds_base) : "memory");
}
template <int N> DI void wait_bar() { asm volatile("s_waitcnt vmcnt(%0) lgkmcnt(0)\n\ts_barrier" ::"n"(N) : "memory"); }
template <int N> DI void wait_vm() { asm volatile("s_waitcnt vmcnt(%0)" ::"n"(N) : "memory"); }
DI void lds_bar() { asm volatile("s_waitcnt lgkmcnt(0)\n\ts_barrier" ::: "memory"); }
DI void raw_bar() { asm volatile("s_barrier" ::: "memory"); }
typedef short v4i16_t __attribute__((ext_vector_type(4)));
DI s16x4 tr_read(const LAS char* p) { return __builtin_bit_cast(s16x4, __builtin_amdgcn_ds_read_tr16_b64_v4i16((LAS v4i16_t*)p)); }
DI bf16x8 cat8(s16x4 a, s16x4 b) { return __builtin_shufflevector(a, b, 0, 1, 2, 3, 4, 5, 6, 7); }
DI void st16_wt(void* p, u32x4 v) { asm volatile("global_store_dwordx4 %0, %1, off sc1\n\ts_nop 1" ::"v"(p), "v"(v) : "memory"); }
DI void st8_wt(void* p, u32x2 v) { asm volatile("global_store_dwordx2 %0, %1, off sc1" ::"v"(p), "v"(v) : "memory"); }
DI void ld2_sc1(u32x4& a, u32x4& b, const char* p0, const char* p1) {
    asm volatile("global_load_dwordx4 %0, %2, off sc1\n\tglobal_load_dwordx4 %1, %3, off sc1" : "=&v"(a), "=&v"(b) : "v"(p0), "v"(p1) : "memory"); }
DI void ld16_sc1(u32x4& a, const char* p) { asm volatile("global_load_dwordx4 %0, %1, off sc1" : "=&v"(a) : "v"(p) : "memory"); }
DI void pin2(u32x4& a, u32x4& b) { asm volatile("" : "+v"(a), "+v"(b)::"memory"); }
DI void ld8_sc1(u32x2& v, const void* p) { asm volatile("global_load_dwordx2 %0, %1, off sc1" : "=&v"(v) : "v"(p) : "memory"); }
DI void ld8_sc1_wait(u32x2& v, const void* p) { asm volatile("global_load_dwordx2 %0, %1, off sc1\n\ts_waitcnt vmcnt(0)" : "=&v"(v) : "v"(p) : "memory"); }
DI void pin1(u32x2& v) { asm volatile("" : "+v"(v)::"memory"); }
template <int N> DI void wait_vm_pin(u32x2& v) { asm volatile("s_waitcnt vmcnt(%1)" : "+v"(v) : "n"(N) : "memory"); }
DI int opaque(int v) { asm volatile("" : "+v"(v)); return v; }
DI float max3f(float a, float b, float c) { float r; asm("v_max3_f32 %0, %1, %2, %3" : "=v"(r) : "v"(a), "v"(b), "v"(c)); return r; }
DI float max2f_pad(float a, float b) { float r; asm("v_max_f32 %0, %1, %2\n\ts_nop 1" : "=v"(r) : "v"(a), "v"(b)); return r; }
template <int CTRL> DI float dpp_add(float v) { return v + __builtin_bit_cast(float, __builtin_amdgcn_update_dpp(0, __builtin_bit_cast(int, v), CTRL, 0xf, 0xf, true)); }
DI float sum8(float v) { v = dpp_add<0xB1>(v); v = dpp_add<0x4E>(v); return dpp_add<0x141>(v); }
DI void poll_flags(u32x2& v, const void* p, bool use, u32x2 tag) {
    unsigned spins = 0;
    while (!__all(!use || (v[0] == tag[0] && v[1] == tag[1]))) {
        __builtin_amdgcn_s_sleep(2);
        ld8_sc1_wait(v, p);
        if (++spins > (1u << 18)) break;
    }
}

#define STAMP_DECL do { } while (0)
#define STAMP(n) do { } while (0)
#define STAMP_END(k) do { } while (0)

constexpr int P1_NSUB = 35, P1_NINT = 18;
constexpr int P2_ND = 28, P2_NS = 36, P2_SL = 7;
constexpr int p2_slot(int i) { constexpr int t[7] = {9, 3, 4, 5, 6, 7, 8}; return t[i % 7]; }
constexpr int p2_unit(int i) {
    constexpr int t[28] = {12, 16, 20, 24, 0, 3, 6, 9, 13, 17, 21, 25, 1, 4, 7, 10, 14, 18, 22, 26, 2, 5, 8, 11, 15, 19, 23, 27};
    return t[i];
}
constexpr int p2_ops(int s) { return s < P2_ND ? 4 : 2; }
constexpr int P2_POLLO = 19, P2_OLD = 24;
constexpr int p2_oissue(int st) { return st < 3 ? P2_OLD : P2_ND + st - 3; }
enum { G_S1 = 0  , G_S2 = 100  , G_KV = 200, G_FH = 300  , G_FQ = 310, G_FO = 311, G_O = 320  ,
       P_BAR = 1000  , P_CHK1 = 1100  , P_C = 1200, P_STEP = 1300  , P_CHK2 = 1400   };
struct LdSim { int pt, grp, cnt; bool seen, done;
    constexpr void op(int g, int n) { if (done) return; if (seen) cnt += n; if (g == grp) { seen = true; cnt = 0; } }
    constexpr void point(int p) { if (p == pt) done = true; } };
constexpr int ld_count(int pt, int grp, bool gemm) {
    LdSim m{pt, grp, 0, false, false};
    for (int i = 0; i < 10; ++i) m.op(G_S1 + i, 4);
    for (int n = 0; n < P1_NINT; ++n) {
        m.point(P_BAR + n);
        if (n == 13) m.point(P_CHK1 + 13);
        if (n == 17) m.point(P_CHK1 + 17);
        if (n == 11) { m.op(G_FQ, 1); if (gemm) m.op(G_FH + 0, 1); }
        if (n == 15 && gemm) m.op(G_FH + 1, 1);
        if (n >= 1) { if (2 * n + 8 < P1_NSUB) m.op(G_S1 + 2 * n + 8, 4); if (2 * n + 9 < P1_NSUB) m.op(G_S1 + 2 * n + 9, 4); }
        if (n == 13 && gemm) m.op(G_S2 + 0, 4);
        if (n == 14) m.op(G_KV + 0, 8);
        if (n == 15) { m.op(G_KV + 1, 4); if (gemm) m.op(G_S2 + 1, 4); }
        if (n == 16 && gemm) { m.op(G_S2 + 2, 4); m.op(G_S2 + 3, 4); }
        if (n == 17 && gemm) { m.op(G_S2 + 4, 4); m.op(G_S2 + 5, 4); }
    }
    m.point(P_C);
    m.op(G_FH + 2, 1); m.op(G_S2 + 6, 4);
    m.point(P_STEP - 1);
    for (int i = 0; i < P2_NS; ++i) {
        m.point(P_STEP + i);
        m.point(P_CHK2 + i);
        if (i + P2_SL < P2_NS) m.op(G_S2 + i + P2_SL, p2_ops(i + P2_SL));
        if (i == 7) m.op(G_FH + 3, 1);
        if (i == P2_POLLO) m.op(G_FO, 1);
        m.point(P_CHK2 + 100 + i);
        for (int st = 0; st < 8; ++st) if (p2_oissue(st) == i) m.op(G_O + st, 2);
    }
    return m.cnt;
}
constexpr int cmin(int a, int b) { return a < b ? a : b; }
constexpr int p1_wait(int n, bool gemm) { return ld_count(P_BAR + n, G_S1 + cmin(2 * n + 3, P1_NSUB - 1), gemm); }
constexpr int p2_wait(int i) {
    int c = ld_count(P_STEP + i, G_S2 + i + 1, true);
    if (i + 1 >= P2_ND && i + 1 < P2_NS) c = cmin(c, ld_count(P_STEP + i, G_O + (i + 1 - P2_ND), true));
    return i + 1 < P2_NS ? c : 0;
}

__global__ __launch_bounds__(768, 3) void fused_kernel(const u16* __restrict__ x, const u16* __restrict__ wqkv, const u16* __restrict__ wo, const u16* __restrict__ wg, const u16* __restrict__ wu,
                                                       const u16* __restrict__ wd, char* __restrict__ ws, u16* __restrict__ out) {
    __shared__ __attribute__((aligned(1024))) char lds_[163840];
    LAS char* lds = (LAS char*)lds_;
    const unsigned lds0 = (unsigned)(uintptr_t)lds;
    const int tid = threadIdx.x, lane = tid & 63, wid = __builtin_amdgcn_readfirstlane(tid >> 6);
    const int bid = blockIdx.x, xcd = bid & 7, idx = bid >> 3;
    const int cg = (xcd & 3) * 2 + (idx & 1), rt = (xcd >> 2) * 16 + (idx >> 1);
    const int g = lane >> 4;
    STAMP_DECL; STAMP(0);
    auto unit_kind = [&](int u) -> int { return u == 0 ? (cg >> 2) : (u == 1 && cg < 4) ? 2 : 3; };
    auto unit_idx = [&](int u) -> int { return u == 0 ? (cg & 3) : (cg < 4) ? (u == 1 ? cg : 3 * cg + u - 2) : 12 + 4 * (cg - 4) + u - 1; };

    if (wid >= 8) {
        const int w = wid - 8;
        __builtin_amdgcn_s_setprio(3);
        const char* wsrc = nullptr; unsigned wstride = 0;
        auto set_unit_src = [&](int u) {
            const int kind = unit_kind(u), ui = unit_idx(u);
            if (kind == 3) { const int c32 = ((lane & 15) >> 1) ^ g; wstride = 3584; wsrc = (const char*)((c32 & 4) ? wu : wg) + ui * 128 + (c32 & 3) * 32 + (lane & 1) * 16; }
            else { const int ch = (((lane & 15) >> 1) ^ (g << 1)) * 2 + (lane & 1); wstride = 2688; wsrc = (const char*)wqkv + kind * 896 + ui * 224 + (ch < 13 ? ch : 13) * 16; }
            wsrc += (size_t)(16 * w + g) * wstride;
        };
        auto issue1 = [&](auto I) {
            constexpr int i = I; constexpr int u = i / 7, ks = i % 7, slot = (i + 4) % 10;
            if constexpr (ks == 0) set_unit_src(u);
#pragma unroll
            for (int j = 0; j < 4; ++j) glds16(wsrc + (size_t)(ks * 64 + 4 * j) * wstride, lds0 + slot * 16384 + w * 4096 + j * 1024);
        };
        u32x2 ev; ld8_sc1(ev, ws + WS_E);
        static_for<0, 6>(issue1);
        wait_vm_pin<24>(ev);
        const unsigned tl = __builtin_amdgcn_readfirstlane(ev[0]) + 1u, th = __builtin_amdgcn_readfirstlane(ev[1]) + (tl == 0u ? 1u : 0u);
        const u32x2 tag = {tl, th};
        if (w == 0 && lane == 0) st8_wt(ws + WS_FS + bid * 8, tag);
        const int head = xcd * 2 + (idx >> 4), qt = idx & 15;
        const bool gemm = idx < 28;
        const int ti = gemm ? xcd * 4 + idx / 7 : 0, cj = gemm ? idx % 7 : 0;
        const int wr = lane >> 3, wsl = lane & 7;
        const int wcb = cj * 128 + ((((wsl >> 1) ^ (wr & 2)) << 1) | (wsl & 1)) * 16;
        const char* wd_src = (const char*)wd + (size_t)(16 * w + wr) * 896 + wcb;
        const char* h_src = ws + WS_H + ((size_t)(ti * 2) * 224 + 2 * w) * 512 + lane * 16;
        const char* wo_src[2];
#pragma unroll
        for (int i = 0; i < 2; ++i) { const int d = (wr < 4 ? 0 : 12) + 4 * (2 * (w & 1) + i) + wr; wo_src[i] = (const char*)wo + (size_t)((w >> 1) * 28 + (d < 28 ? d : 27)) * 896 + wcb; }
        const unsigned ring = lds0 + w * 4096;
        auto issue2 = [&](auto I) {
            constexpr int i = I; constexpr int so = p2_slot(i) * 16384;
            if constexpr (i < P2_ND) {
                constexpr int u = p2_unit(i);
                glds16_sc1(h_src + (size_t)u * 4096, ring + so);
                glds16_sc1(h_src + (size_t)u * 4096 + 224 * 512, ring + so + 1024);
                glds16(wd_src + (size_t)u * 64 * 896, ring + so + 2048);
                glds16(wd_src + (size_t)(u * 64 + 8) * 896, ring + so + 3072);
            } else {
                glds16(wo_src[0] + (size_t)(i - P2_ND) * 56 * 896, ring + so + 2048);
                glds16(wo_src[1] + (size_t)(i - P2_ND) * 56 * 896, ring + so + 3072);
            }
        };
        auto issue_kv = [&](int j0, int j1) {
            for (int j = j0; j < j1; ++j) {
                const int k = 4 * j + w, lt = k < 24 ? k : k - 24; int at = qt * 8 - 16 + lt; at = at < 0 ? 0 : at;
                glds16_sc1(ws + (k < 24 ? WS_K : WS_V) + (size_t)(head * 128 + at) * 1024 + lane * 16, lds0 + k * 1024);
            }
        };
        int fu = 0;
        static_for<0, 28>([&](auto I) { if ((lane >> 1) == (int)I) fu = p2_unit(I); });
        const char* hflag = ws + WS_FH + ((size_t)((ti * 4 + w) * 32 + fu) * 2 + (lane & 1)) * 8;
        const char* oflag = ws + WS_FO + ((size_t)(ti * 2 + (w >> 1)) * 32 + (lane & 31)) * 8;
        const char* kflag;
        { const int li = lane < 12 ? lane : lane < 24 ? lane - 12 : 0; int r = 2 * qt - 4 + (li >> 1); r = r < 0 ? 0 : r;
          kflag = ws + WS_FQ + ((size_t)((head * 3 + (lane < 12 ? 1 : 2)) * 32 + r) * 2 + (li & 1)) * 8; }
        u32x2 fv, fq;
        raw_bar();
        raw_bar();
        static_for<6, 10>(issue1);
        auto p1_loop = [&](auto GEMM) {
            constexpr bool GM = GEMM;
            static_for<0, P1_NINT>([&](auto N) {
                constexpr int n = N;
                wait_vm<p1_wait(n, GM)>();
                STAMP(20 + n);
                raw_bar();
                if constexpr (n == 13) {
                    if constexpr (GM) { wait_vm_pin<ld_count(P_CHK1 + 13, G_FH + 0, true)>(fv); pin1(fq); } else wait_vm_pin<ld_count(P_CHK1 + 13, G_FQ, false)>(fq);
                    poll_flags(fq, kflag, lane < 24, tag);
                    if constexpr (GM) poll_flags(fv, hflag, lane < 8, tag);
                }
                if constexpr (n == 17 && GM) { wait_vm_pin<ld_count(P_CHK1 + 17, G_FH + 1, true)>(fv); poll_flags(fv, hflag, lane < 24, tag); }
                if constexpr (n == 11) { ld8_sc1(fq, kflag); if constexpr (GM) { ld8_sc1(fv, hflag); } }
                if constexpr (n == 15 && GM) ld8_sc1(fv, hflag);
                if constexpr (n >= 1 && 2 * n + 8 < P1_NSUB) issue1(ic<(n >= 1 && 2 * n + 8 < P1_NSUB) ? 2 * n + 8 : 0>{});
                if constexpr (n >= 1 && 2 * n + 9 < P1_NSUB) issue1(ic<(n >= 1 && 2 * n + 9 < P1_NSUB) ? 2 * n + 9 : 0>{});
                if constexpr (n == 13 && GM) issue2(ic<0>{});
                if constexpr (n == 14) issue_kv(0, 8);
                if constexpr (n == 15) { issue_kv(8, 12); if constexpr (GM) issue2(ic<1>{}); }
                if constexpr (n == 16 && GM) { issue2(ic<2>{}); issue2(ic<3>{}); }
                if constexpr (n == 17 && GM) { issue2(ic<4>{}); issue2(ic<5>{}); }
            });
            wait_vm<ld_count(P_C, G_KV + 1, GM)>();
            raw_bar();
        };
        if (gemm) p1_loop(std::true_type{}); else p1_loop(std::false_type{});
        STAMP(1);
        if (!gemm) {
            if (bid == 255 && w == 0) {
                unsigned spins = 0;
                for (;;) {
                    bool ok = true;
#pragma unroll
                    for (int k = 0; k < 4; ++k) { u32x2 v; ld8_sc1_wait(v, ws + WS_FS + (k * 64 + lane) * 8); ok = ok && v[0] == tag[0] && v[1] == tag[1]; }
                    if (__all(ok) || ++spins > (1u << 18)) break;
                    __builtin_amdgcn_s_sleep(8);
                }
                if (lane == 0) st8_wt(ws + WS_E, tag);
            }
            return;
        }
        const int t32 = lane & 31, hh = lane >> 5, q4 = (lane & 15) >> 2, p4 = lane & 3;
        ld8_sc1(fv, hflag); issue2(ic<6>{});
        STAMP(2);
        f32x16 acc[2][2];
#pragma unroll
        for (int a = 0; a < 2; ++a)
#pragma unroll
            for (int b = 0; b < 2; ++b)
#pragma unroll
                for (int r = 0; r < 16; ++r) acc[a][b][r] = 0.f;
        const LAS char* rb = lds + w * 4096;
        unsigned wro[2];
#pragma unroll
        for (int ct = 0; ct < 2; ++ct) wro[ct] = 2048 + (8 * hh + q4) * 128 + (((2 * (ct ^ (w & 1)) + (g & 1)) ^ (q4 & 2)) << 5) + p4 * 8;
        const unsigned hro0 = (w >> 1) * 1024 + lane * 16, hro1 = (1 - (w >> 1)) * 1024 + lane * 16;
        bf16x8 wfr[2][2], hfr[2][2];
        u32x4 of[3][2];
        const char* op = ws + WS_O + ((size_t)(ti * 2 * 16 + (w >> 1)) * 4 + 2 * (w & 1)) * 512 + lane * 16;
        auto rd_stage = [&](auto I) {
            constexpr int i = I; constexpr int so = p2_slot(i) * 16384, b = i & 1;
            const LAS char* sb = rb + so;
#pragma unroll
            for (int ct = 0; ct < 2; ++ct) wfr[b][ct] = cat8(tr_read(sb + wro[ct]), tr_read(sb + wro[ct] + 512));
            if constexpr (i < P2_ND) { hfr[b][0] = *(const LAS bf16x8*)(sb + hro0); hfr[b][1] = *(const LAS bf16x8*)(sb + hro1); }
        };
        auto oload = [&](auto ST) { constexpr int st = ST; ld2_sc1(of[st % 3][0], of[st % 3][1], op + st * 4096 + (w >> 1) * 32768, op + st * 4096 + (1 - (w >> 1)) * 32768); };
        LAS f32x4* scr = (LAS f32x4*)lds;
        f32x16 gfin;
        auto reduce = [&](f32x16& res) {
#pragma unroll
            for (int d = 1; d < 4; ++d) {
                const f32x16& a = acc[d & 1][d >> 1];
#pragma unroll
                for (int k = 0; k < 4; ++k) scr[((w * 3 + d - 1) * 4 + k) * 64 + lane] = f32x4{a[4 * k], a[4 * k + 1], a[4 * k + 2], a[4 * k + 3]};
            }
            lds_bar();
            res = acc[0][0];
#pragma unroll
            for (int d = 1; d < 4; ++d)
#pragma unroll
                for (int k = 0; k < 4; ++k) { const f32x4 v = scr[(((w ^ d) * 3 + d - 1) * 4 + k) * 64 + lane]; res[4 * k] += v[0]; res[4 * k + 1] += v[1]; res[4 * k + 2] += v[2]; res[4 * k + 3] += v[3]; }
        };
        wait_vm<ld_count(P_STEP - 1, G_S2 + 0, true)>();
        rd_stage(ic<0>{});
        static_for<0, P2_NS>([&](auto I) {
            constexpr int i = I; constexpr int b = i & 1;
            asm volatile("s_waitcnt vmcnt(%0) lgkmcnt(0)" ::"n"(p2_wait(i)) : "memory");
            if constexpr (i == 5) { wait_vm_pin<ld_count(P_CHK2 + 5, G_FH + 2, true)>(fv); poll_flags(fv, hflag, lane < 40, tag); }
            if constexpr (i == 13) { wait_vm_pin<ld_count(P_CHK2 + 13, G_FH + 3, true)>(fv); poll_flags(fv, hflag, lane < 56, tag); }
            if constexpr (i + P2_SL < P2_NS) issue2(ic<(i + P2_SL < P2_NS ? i + P2_SL : 0)>{});
            if constexpr (i == 7) ld8_sc1(fv, hflag);
            if constexpr (i == P2_POLLO) ld8_sc1(fv, oflag);
            if constexpr (i == P2_OLD) {
                wait_vm_pin<ld_count(P_CHK2 + 100 + P2_OLD, G_FO, true)>(fv);
                poll_flags(fv, oflag, lane < 32, tag);
                raw_bar();
                oload(ic<0>{}); oload(ic<1>{}); oload(ic<2>{});
            }
            if constexpr (i + 1 < P2_NS) rd_stage(ic<(i + 1 < P2_NS ? i + 1 : 0)>{});
            if constexpr ((i & 3) == 0) { STAMP(10 + i / 4); }
            if constexpr (i < P2_ND) {
#pragma unroll
                for (int ct = 0; ct < 2; ++ct)
#pragma unroll
                    for (int tt = 0; tt < 2; ++tt) acc[ct][tt] = MFMA32(wfr[b][ct], hfr[b][tt], acc[ct][tt]);
                if constexpr (i == P2_ND - 1) {
                    reduce(gfin);
#pragma unroll
                    for (int a = 0; a < 2; ++a)
#pragma unroll
                        for (int c = 0; c < 2; ++c)
#pragma unroll
                            for (int r = 0; r < 16; ++r) acc[a][c][r] = 0.f;
                }
            } else {
                constexpr int st = i - P2_ND;
                pin2(of[st % 3][0], of[st % 3][1]);
#pragma unroll
                for (int ct = 0; ct < 2; ++ct)
#pragma unroll
                    for (int tt = 0; tt < 2; ++tt) acc[ct][tt] = MFMA32(wfr[b][ct], __builtin_bit_cast(bf16x8, of[st % 3][tt]), acc[ct][tt]);
                if constexpr (st + 3 < 8) oload(ic<(st + 3 < 8) ? st + 3 : 0>{});
            }
        });
        STAMP(7);
        const int tok = ti * 64 + (w >> 1) * 32 + t32, col0 = cj * 64 + (w & 1) * 32 + 8 * hh;
        const u32x4 xa = *(const GAS u32x4*)((const GAS char*)x + (size_t)tok * 896 + col0 * 2), xb = *(const GAS u32x4*)((const GAS char*)x + (size_t)tok * 896 + col0 * 2 + 32);
        lds_bar();
        f32x16 sfin;
        reduce(sfin);
        unsigned sp[8];
#pragma unroll
        for (int j = 0; j < 8; ++j) {
            float r2[2];
#pragma unroll
            for (int e = 0; e < 2; ++e) { const float f = rbf(gfin[2 * j + e]), gt = rbf(f * __builtin_amdgcn_rcpf(1.0f + __expf(-f))); r2[e] = rbf(sfin[2 * j + e]) * gt; }
            sp[j] = pk_bf16(r2[0], r2[1]);
        }
#pragma unroll
        for (int n = 0; n < 2; ++n) {
            const auto r0 = __builtin_amdgcn_permlane32_swap(sp[4 * n], sp[4 * n + 2], false, false), r1 = __builtin_amdgcn_permlane32_swap(sp[4 * n + 1], sp[4 * n + 3], false, false);
            const u32x4 sg = {r0[0], r1[0], r0[1], r1[1]}, xx = n ? xb : xa;
            u32x4 o;
#pragma unroll
            for (int j = 0; j < 4; ++j) o[j] = pk_bf16(lo_f(xx[j]) + lo_f(sg[j]), hi_f(xx[j]) + hi_f(sg[j]));
            *(GAS u32x4*)((GAS char*)out + (size_t)tok * 896 + col0 * 2 + n * 32) = o;
        }
        STAMP(8);
        if (w == 0) STAMP_END(1);
        return;
    }

    const int wm = wid >> 2, wn = wid & 3;
    const int t32 = lane & 31, hh = lane >> 5;
    u32x4 xv[7]; u32x2 ev; ld8_sc1(ev, ws + WS_E);
    {
        const char* xp = (const char*)x + (size_t)(rt * 64 + (tid >> 3)) * 896 + (tid & 7) * 16;
        asm volatile("global_load_dwordx4 %0, %7, off\n\tglobal_load_dwordx4 %1, %7, off offset:128\n\tglobal_load_dwordx4 %2, %7, off offset:256\n\tglobal_load_dwordx4 %3, %7, off offset:384\n\t"
                     "global_load_dwordx4 %4, %7, off offset:512\n\tglobal_load_dwordx4 %5, %7, off offset:640\n\tglobal_load_dwordx4 %6, %7, off offset:768"
                     : "=&v"(xv[0]), "=&v"(xv[1]), "=&v"(xv[2]), "=&v"(xv[3]), "=&v"(xv[4]), "=&v"(xv[5]), "=&v"(xv[6]) : "v"(xp) : "memory");
    }
    asm volatile("s_waitcnt vmcnt(0)" : "+v"(ev), "+v"(xv[0]), "+v"(xv[1]), "+v"(xv[2]), "+v"(xv[3]), "+v"(xv[4]), "+v"(xv[5]), "+v"(xv[6])::"memory");
    const unsigned tl_ = __builtin_amdgcn_readfirstlane(ev[0]) + 1u, th_ = __builtin_amdgcn_readfirstlane(ev[1]) + (tl_ == 0u ? 1u : 0u);
    const u32x2 tag = {tl_, th_};
    STAMP(1);
    {
        float v[56]; float s0 = 0.f, s1 = 0.f;
#pragma unroll
        for (int i = 0; i < 7; ++i)
#pragma unroll
            for (int j = 0; j < 4; ++j) { v[i * 8 + 2 * j] = lo_f(xv[i][j]); v[i * 8 + 2 * j + 1] = hi_f(xv[i][j]); s0 += v[i * 8 + 2 * j]; s1 += v[i * 8 + 2 * j + 1]; }
        const float mean = sum8(s0 + s1) * (1.0f / 448.f), mb = rbf(mean);
        float q0 = 0.f, q1 = 0.f;
#pragma unroll
        for (int i = 0; i < 28; ++i) { v[2 * i] -= mb; v[2 * i + 1] -= mb; q0 = __builtin_fmaf(v[2 * i], v[2 * i], q0); q1 = __builtin_fmaf(v[2 * i + 1], v[2 * i + 1], q1); }
        const float dm = mean - mb, var = __builtin_fmaf(-dm, dm, sum8(q0 + q1) * (1.0f / 448.f));
        const float den = rbf(__builtin_sqrtf(rbf(rbf(var) + 1e-5f))), rden = 1.0f / den;
        const int row = tid >> 3, c8 = tid & 7;
        LAS char* dst = lds + (((row >> 5) * 28 + (c8 >> 1)) * 64 + (c8 & 1) * 32 + ((row & 31) ^ c8)) * 16;
#pragma unroll
        for (int i = 0; i < 7; ++i) {
            u32x4 o;
#pragma unroll
            for (int j = 0; j < 4; ++j) { const unsigned dp = pk_bf16(v[i * 8 + 2 * j], v[i * 8 + 2 * j + 1]); o[j] = pk_bf16(lo_f(dp) * rden, hi_f(dp) * rden); }
            *(LAS u32x4*)(dst + i * 4096) = o;
        }
    }
    lds_bar();
    bf16x8 xf[28];
#pragma unroll
    for (int s = 0; s < 28; ++s) xf[s] = *(const LAS bf16x8*)(lds + ((wm * 28 + s) * 64 + hh * 32 + (t32 ^ ((2 * s + hh) & 7))) * 16);
    lds_bar();
    STAMP(2);
    {
        f32x16 acc;
        unsigned rdo[3];
        auto set_rd = [&](int u) {
            const int ln = opaque(lane), hh = ln >> 5, q4 = (ln & 15) >> 2, p4 = ln & 3, chh = (ln >> 4) & 1;
            const int kind = unit_kind(u);
            const int byte = (kind == 3) ? (chh * 4 + wn) * 32 + 8 * p4 : 56 * wn + 32 * chh + 8 * ((chh && p4 == 3) ? 2 : p4);
            const int sw = (kind == 3) ? q4 : (q4 << 1);
            rdo[0] = (unsigned)((8 * hh + q4) * 256 + ((((byte >> 5) ^ sw) << 5) | (byte & 31))); rdo[1] = rdo[0] + 65536u; rdo[2] = rdo[0] + 131072u;
        };
        bf16x8 wf[3];
        auto rd_frag = [&](auto FF) {
            constexpr int Fi = FF; constexpr int i = Fi / 4, kq = Fi % 4, slot = (i + 4) % 10, off = slot * 16384 + kq * 4096;
            if constexpr (Fi % 28 == 0) set_rd(i / 7);
            const LAS char* b = lds + rdo[off >> 16] + (off & 65535);
            wf[Fi % 3] = cat8(tr_read(b), tr_read(b + 1024));
        };
        auto flag_unit = [&](int u) {
            const int kind = unit_kind(u), ui = unit_idx(u);
            const int lane = opaque(tid) & 63;
            char* f = (kind == 3) ? ws + WS_FH + ((size_t)((rt * 4 + wn) * 32 + ui) * 2 + wm) * 8 : ws + WS_FQ + ((size_t)(((4 * ui + wn) * 3 + kind) * 32 + rt) * 2 + wm) * 8;
            if (lane == 0) st8_wt(f, tag);
        };
        auto epilogue = [&](auto U) {
            constexpr int u = U;
            const int kind = unit_kind(u), ui = unit_idx(u);
            const int ln = opaque(tid) & 63, t32 = ln & 31, hh = ln >> 5;
            if (kind == 3) {
                unsigned hp[4];
#pragma unroll
                for (int j = 0; j < 4; ++j) {
                    const unsigned gp = pk_bf16(acc[2 * j], acc[2 * j + 1]), up = pk_bf16(acc[8 + 2 * j], acc[8 + 2 * j + 1]);
                    hp[j] = pk_bf16(lo_f(gp) * lo_f(up), hi_f(gp) * hi_f(up));
                }
                const auto r0 = __builtin_amdgcn_permlane32_swap(hp[0], hp[2], false, false), r1 = __builtin_amdgcn_permlane32_swap(hp[1], hp[3], false, false);
                st16_wt(ws + WS_H + ((size_t)((rt * 2 + wm) * 224 + ui * 8 + wn * 2 + hh) * 32 + t32) * 16, u32x4{r0[0], r1[0], r0[1], r1[1]});
            } else {
                unsigned p[8];
#pragma unroll
                for (int j = 0; j < 8; ++j) p[j] = pk_bf16(acc[2 * j], acc[2 * j + 1]);
                if (hh) { p[6] = (kind == 2) ? 0x00003f80u : 0u; p[7] = 0u; }
                char* base = ws + (kind == 0 ? WS_Q : kind == 1 ? WS_K : WS_V) + (size_t)((4 * ui + wn) * 128 + rt * 4 + wm * 2 + (t32 >> 4)) * 1024;
                const int tt = (kind == 2) ? ((t32 & 15) ^ (8 * hh)) : (t32 & 15);
#pragma unroll
                for (int n = 0; n < 2; ++n) {
                    const auto r0 = __builtin_amdgcn_permlane32_swap(p[4 * n], p[4 * n + 2], false, false), r1 = __builtin_amdgcn_permlane32_swap(p[4 * n + 1], p[4 * n + 3], false, false);
                    st16_wt(base + ((2 * n + hh) * 16 + tt) * 16, u32x4{r0[0], r1[0], r0[1], r1[1]});
                }
            }
        };
        const int head = xcd * 2 + (idx >> 4), qt = idx & 15, a = wid;
        u32x2 fq; u32x4 qv;
        const int tq = qt * 8 + a;
        const char* qflag = ws + WS_FQ + ((size_t)((head * 3) * 32 + (tq >> 2)) * 2 + ((tq >> 1) & 1)) * 8;
        const char* qsrc = ws + WS_Q + (size_t)(head * 128 + tq) * 1024 + (opaque(tid) & 63) * 16;
        raw_bar();
        rd_frag(ic<0>{}); rd_frag(ic<1>{});
        static_for<0, 4 * P1_NSUB>([&](auto FF) {
            constexpr int Fi = FF;
            if constexpr (Fi % 8 == 0 && Fi > 0) { raw_bar(); if constexpr (Fi / 8 < 18) { STAMP(10 + Fi / 8); } }
            if constexpr (Fi + 2 < 4 * P1_NSUB) rd_frag(ic<(Fi + 2 < 4 * P1_NSUB ? Fi + 2 : 0)>{});
            if constexpr (Fi % 28 == 0) {
#pragma unroll
                for (int r = 0; r < 16; ++r) acc[r] = 0.f;
            }
            acc = MFMA32(wf[Fi % 3], xf[Fi % 28], acc);
            if constexpr (Fi % 28 == 27) epilogue(ic<Fi / 28>{});
            if constexpr (Fi % 28 == 7 && Fi >= 28) { wait_vm<0>(); flag_unit(Fi / 28 - 1); }
            if constexpr (Fi == 112 + 16) ld8_sc1(fq, qflag);
            if constexpr (Fi == 112 + 24) { wait_vm_pin<0>(fq); poll_flags(fq, qflag, true, tag); ld16_sc1(qv, qsrc); }
        });
        STAMP(3);
        asm volatile("s_waitcnt vmcnt(%1) lgkmcnt(0)\n\ts_barrier" : "+v"(qv) : "n"(1) : "memory");
        const int lane = opaque(tid) & 63, g = lane >> 4, q4 = (lane & 15) >> 2, p4 = lane & 3;
        constexpr unsigned LK = 0, LV = 24576;
        STAMP(4);
        const int t16 = lane & 15;
        const bf16x8 qf = __builtin_bit_cast(bf16x8, qv);
        f32x4 sc[17];
#pragma unroll
        for (int j = 0; j < 17; ++j) { const bf16x8 kf = *(const LAS bf16x8*)(lds + LK + (a + j) * 1024 + lane * 16); sc[j] = MFMA16(kf, qf, (f32x4{0.f, 0.f, 0.f, 0.f})); }
        SBAR(); asm volatile("s_nop 15"); SBAR();
        const float NEG = -1e30f, L2E = 1.4426950408889634f, SCALE = 0.18898223650461362f;
#pragma unroll
        for (int r = 0; r < 4; ++r) { if (4 * g + r <= t16) sc[0][r] = NEG; if (4 * g + r > t16) sc[16][r] = NEG; }
        if (qt < 2) {
            asm volatile("" ::: "memory");
            const int first = 16 - qt * 8 - a;
#pragma unroll
            for (int j = 0; j < 16; ++j) if (j < first) sc[j] = f32x4{NEG, NEG, NEG, NEG};
        }
        float mx = max3f(sc[0][0], sc[0][1], sc[0][2]);
        mx = max3f(mx, sc[0][3], sc[1][0]);
        mx = max3f(mx, sc[1][1], sc[1][2]);
#pragma unroll
        for (int j = 2; j < 16; ++j) { mx = max3f(mx, sc[j][0], sc[j][1]); mx = max3f(mx, sc[j][2], sc[j][3]); }
        mx = max3f(mx, sc[16][0], sc[16][1]); mx = max3f(mx, sc[16][2], sc[16][3]);
        mx = max2f_pad(mx, sc[1][3]);
        { const auto r = __builtin_amdgcn_permlane16_swap(__float_as_uint(mx), __float_as_uint(mx), false, false); mx = max2f_pad(__uint_as_float(r[0]), __uint_as_float(r[1])); }
        { const auto r = __builtin_amdgcn_permlane32_swap(__float_as_uint(mx), __float_as_uint(mx), false, false); mx = max2f_pad(__uint_as_float(r[0]), __uint_as_float(r[1])); }
        wait_vm<0>(); flag_unit(4);
        const float nm = -rbf(rbf(mx) * SCALE) * L2E;
        const f32x2 nm2 = {nm, nm}, l2 = {L2E, L2E}, sc2 = {SCALE, SCALE};
        u32x2 pp[18];
#pragma unroll
        for (int j = 0; j < 17; ++j) {
            unsigned pk[2];
#pragma unroll
            for (int r = 0; r < 2; ++r) {
                const unsigned p1 = pk_bf16(sc[j][2 * r], sc[j][2 * r + 1]);
                const f32x2 s1 = f32x2{lo_f(p1), hi_f(p1)} * sc2;
                const unsigned p2 = pk_bf16(s1[0], s1[1]);
                const f32x2 e = __builtin_elementwise_fma(f32x2{lo_f(p2), hi_f(p2)}, l2, nm2);
                pk[r] = pk_bf16(__builtin_amdgcn_exp2f(e[0]), __builtin_amdgcn_exp2f(e[1]));
            }
            pp[j] = u32x2{pk[0], pk[1]};
        }
        pp[17] = u32x2{0u, 0u};
        f32x4 o0 = {0.f, 0.f, 0.f, 0.f}, o1 = {0.f, 0.f, 0.f, 0.f};
        const int vkey = 4 * g + q4;
        const unsigned va0 = LV + a * 1024 + (((p4 >> 1) * 16 + (vkey ^ (8 * (p4 >> 1)))) * 16) + (p4 & 1) * 8;
#pragma unroll
        for (int jp = 0; jp < 9; ++jp) {
            const int ja = 2 * jp, jb = (jp < 8) ? 2 * jp + 1 : 2 * jp;
            const bf16x8 pf = __builtin_bit_cast(bf16x8, (u32x4{pp[2 * jp][0], pp[2 * jp][1], pp[2 * jp + 1][0], pp[2 * jp + 1][1]}));
            const bf16x8 v0 = cat8(tr_read(lds + va0 + ja * 1024), tr_read(lds + va0 + jb * 1024));
            const bf16x8 v1 = cat8(tr_read(lds + va0 + ja * 1024 + 512), tr_read(lds + va0 + jb * 1024 + 512));
            o0 = MFMA16(v0, pf, o0); o1 = MFMA16(v1, pf, o1);
        }
        const float l = __shfl(o1[0], 48 + t16), rl = 1.0f / l;
        u32x4 ov = {pk_bf16(o0[0] * rl, o0[1] * rl), pk_bf16(o0[2] * rl, o0[3] * rl), pk_bf16(o1[0] * rl, o1[1] * rl), pk_bf16(o1[2] * rl, o1[3] * rl)};
        if (g == 3) { ov[2] = 0u; ov[3] = 0u; }
        const int tl = qt * 8 + a;
        STAMP(5);
        st16_wt(ws + WS_O + ((size_t)((tl >> 1) * 16 + head) * 4 + g) * 512 + ((tl & 1) * 16 + t16) * 16, ov);
        wait_vm<0>();
        if (lane == 0) st8_wt(ws + WS_FO + ((size_t)(((tl >> 2) * 2 + (head & 1)) * 8 + (head >> 1)) * 4 + (tl & 3)) * 8, tag);
        STAMP(6);
        if (wid == 0) STAMP_END(0);
    }
}

extern "C" void kernel_launch(void* const* d_in, const int* in_sizes, int n_in, void* d_out, int out_size, void* d_ws, size_t ws_size, hipStream_t stream) {
    const u16* x = (const u16*)d_in[0]; const u16* wqkv = (const u16*)d_in[1]; const u16* wo = (const u16*)d_in[2];
    const u16* wg = (const u16*)d_in[4]; const u16* wu = (const u16*)d_in[5]; const u16* wd = (const u16*)d_in[6];
    fused_kernel<<<256, 768, 0, stream>>>(x, wqkv, wo, wg, wu, wd, (char*)d_ws, (u16*)d_out);
}
```

```cpp
#define WARM 1
#define NO_PRIO 1
#include <hip/hip_runtime.h>
#include <stdint.h>
#include <type_traits>

typedef unsigned short u16;
typedef unsigned long long u64;
typedef short bf16x8 __attribute__((ext_vector_type(8)));
typedef short s16x4 __attribute__((ext_vector_type(4)));
typedef float f32x4 __attribute__((ext_vector_type(4)));
typedef float f32x16 __attribute__((ext_vector_type(16)));
typedef float f32x2 __attribute__((ext_vector_type(2)));
typedef unsigned u32x4 __attribute__((ext_vector_type(4)));
typedef unsigned u32x2 __attribute__((ext_vector_type(2)));
typedef __bf16 bf2 __attribute__((ext_vector_type(2)));
#define LAS __attribute__((address_space(3)))
#define GAS __attribute__((address_space(1)))
#define DI __device__ __forceinline__

constexpr size_t WS_E = 0;
constexpr size_t WS_FS = 4096;
constexpr size_t WS_FQ = 8192;
constexpr size_t WS_FH = 32768;
constexpr size_t WS_FO = 98304;
constexpr size_t WS_Q = 131072;
constexpr size_t WS_K = WS_Q + 2097152;
constexpr size_t WS_V = WS_K + 2097152;
constexpr size_t WS_H = WS_V + 2097152;
constexpr size_t WS_O = WS_H + 7340032;
constexpr size_t WS_END = WS_O + 2097152;

template <int N> using ic = std::integral_constant<int, N>;
template <int B, int E, class Fn> DI void static_for(Fn&& f) { if constexpr (B < E) { f(ic<B>{}); static_for<B + 1, E>(f); } }

DI unsigned pk_bf16(float a, float b) { f32x2 v = {a, b}; bf2 r = __builtin_convertvector(v, bf2); return __builtin_bit_cast(unsigned, r); }
DI float lo_f(unsigned p) { return __uint_as_float(p << 16); }
DI float hi_f(unsigned p) { return __uint_as_float(p & 0xffff0000u); }
DI float rbf(float a) { return lo_f(pk_bf16(a, 0.f)); }
#define MFMA16(a, b, c) __builtin_amdgcn_mfma_f32_16x16x32_bf16((a), (b), (c), 0, 0, 0)
#define MFMA32(a, b, c) __builtin_amdgcn_mfma_f32_32x32x16_bf16((a), (b), (c), 0, 0, 0)
#define SBAR() __builtin_amdgcn_sched_barrier(0)
DI void glds16(const void* g, unsigned lds_base) {
    unsigned sv;
    asm volatile("s_mov_b32 %0, m0\n\ts_mov_b32 m0, %2\n\ts_nop 0\n\tglobal_load_lds_dwordx4 %1, off\n\ts_mov_b32 m0, %0" : "=&s"(sv) : "v"(g), "s"(lds_base) : "memory");
}
DI void glds16_sc1(const void* g, unsigned lds_base) {
    unsigned sv;
    asm volatile("s_mov_b32 %0, m0\n\ts_mov_b32 m0, %2\n\ts_nop 0\n\tglobal_load_lds_dwordx4 %1, off sc1\n\ts_mov_b32 m0, %0" : "=&s"(sv) : "v"(g), "s"(lds_base) : "memory");
}
template <int N> DI void wait_bar() { asm volatile("s_waitcnt vmcnt(%0) lgkmcnt(0)\n\ts_barrier" ::"n"(N) : "memory"); }
template <int N> DI void wait_vm() { asm volatile("s_waitcnt vmcnt(%0)" ::"n"(N) : "memory"); }
DI void lds_bar() { asm volatile("s_waitcnt lgkmcnt(0)\n\ts_barrier" ::: "memory"); }
DI void raw_bar() { asm volatile("s_barrier" ::: "memory"); }
typedef short v4i16_t __attribute__((ext_vector_type(4)));
DI s16x4 tr_read(const LAS char* p) { return __builtin_bit_cast(s16x4, __builtin_amdgcn_ds_read_tr16_b64_v4i16((LAS v4i16_t*)p)); }
DI bf16x8 cat8(s16x4 a, s16x4 b) { return __builtin_shufflevector(a, b, 0, 1, 2, 3, 4, 5, 6, 7); }
DI void st16_wt(void* p, u32x4 v) { asm volatile("global_store_dwordx4 %0, %1, off sc1\n\ts_nop 1" ::"v"(p), "v"(v) : "memory"); }
DI void st8_wt(void* p, u32x2 v) { asm volatile("global_store_dwordx2 %0, %1, off sc1" ::"v"(p), "v"(v) : "memory"); }
DI void ld2_sc1(u32x4& a, u32x4& b, const char* p0, const char* p1) {
    asm volatile("global_load_dwordx4 %0, %2, off sc1\n\tglobal_load_dwordx4 %1, %3, off sc1" : "=&v"(a), "=&v"(b) : "v"(p0), "v"(p1) : "memory"); }
DI void ld16_sc1(u32x4& a, const char* p) { asm volatile("global_load_dwordx4 %0, %1, off sc1" : "=&v"(a) : "v"(p) : "memory"); }
DI void pin2(u32x4& a, u32x4& b) { asm volatile("" : "+v"(a), "+v"(b)::"memory"); }
DI void ld8_sc1(u32x2& v, const void* p) { asm volatile("global_load_dwordx2 %0, %1, off sc1" : "=&v"(v) : "v"(p) : "memory"); }
DI void ld8_sc1_wait(u32x2& v, const void* p) { asm volatile("global_load_dwordx2 %0, %1, off sc1\n\ts_waitcnt vmcnt(0)" : "=&v"(v) : "v"(p) : "memory"); }
DI void pin1(u32x2& v) { asm volatile("" : "+v"(v)::"memory"); }
template <int N> DI void wait_vm_pin(u32x2& v) { asm volatile("s_waitcnt vmcnt(%1)" : "+v"(v) : "n"(N) : "memory"); }
DI int opaque(int v) { asm volatile("" : "+v"(v)); return v; }
DI float max3f(float a, float b, float c) { float r; asm("v_max3_f32 %0, %1, %2, %3" : "=v"(r) : "v"(a), "v"(b), "v"(c)); return r; }
DI float max2f_pad(float a, float b) { float r; asm("v_max_f32 %0, %1, %2\n\ts_nop 1" : "=v"(r) : "v"(a), "v"(b)); return r; }
template <int CTRL> DI float dpp_add(float v) { return v + __builtin_bit_cast(float, __builtin_amdgcn_update_dpp(0, __builtin_bit_cast(int, v), CTRL, 0xf, 0xf, true)); }
DI float sum8(float v) { v = dpp_add<0xB1>(v); v = dpp_add<0x4E>(v); return dpp_add<0x141>(v); }
DI void poll_flags(u32x2& v, const void* p, bool use, u32x2 tag) {
    unsigned spins = 0;
    while (!__all(!use || (v[0] == tag[0] && v[1] == tag[1]))) {
        __builtin_amdgcn_s_sleep(2);
        ld8_sc1_wait(v, p);
        if (++spins > (1u << 18)) break;
    }
}

#define STAMP_DECL do { } while (0)
#define STAMP(n) do { } while (0)
#define STAMP_END(k) do { } while (0)

constexpr int P1_NSUB = 35, P1_NINT = 18;
constexpr int P2_ND = 28, P2_NS = 36, P2_SL = 7;
constexpr int p2_slot(int i) { constexpr int t[7] = {9, 3, 4, 5, 6, 7, 8}; return t[i % 7]; }
constexpr int p2_unit(int i) {
    constexpr int t[28] = {12, 16, 20, 24, 0, 3, 6, 9, 13, 17, 21, 25, 1, 4, 7, 10, 14, 18, 22, 26, 2, 5, 8, 11, 15, 19, 23, 27};
    return t[i];
}
constexpr int p2_ops(int s) { return s < P2_ND ? 4 : 2; }
constexpr int P2_POLLO = 19, P2_OLD = 24;
constexpr int p2_oissue(int st) { return st < 3 ? P2_OLD : P2_ND + st - 3; }
enum { G_S1 = 0  , G_S2 = 100  , G_KV = 200, G_FH = 300  , G_FQ = 310, G_FO = 311, G_O = 320  ,
       P_BAR = 1000  , P_CHK1 = 1100  , P_C = 1200, P_STEP = 1300  , P_CHK2 = 1400   };
struct LdSim { int pt, grp, cnt; bool seen, done;
    constexpr void op(int g, int n) { if (done) return; if (seen) cnt += n; if (g == grp) { seen = true; cnt = 0; } }
    constexpr void point(int p) { if (p == pt) done = true; } };
constexpr int ld_count(int pt, int grp, bool gemm) {
    LdSim m{pt, grp, 0, false, false};
    for (int i = 0; i < 10; ++i) m.op(G_S1 + i, 4);
    for (int n = 0; n < P1_NINT; ++n) {
        m.point(P_BAR + n);
        if (n == 13) m.point(P_CHK1 + 13);
        if (n == 17) m.point(P_CHK1 + 17);
        if (n == 11) { m.op(G_FQ, 1); if (gemm) m.op(G_FH + 0, 1); }
        if (n == 15 && gemm) m.op(G_FH + 1, 1);
        if (n >= 1) { if (2 * n + 8 < P1_NSUB) m.op(G_S1 + 2 * n + 8, 4); if (2 * n + 9 < P1_NSUB) m.op(G_S1 + 2 * n + 9, 4); }
        if (n == 13 && gemm) m.op(G_S2 + 0, 4);
        if (n == 14) m.op(G_KV + 0, 8);
        if (n == 15) { m.op(G_KV + 1, 4); if (gemm) m.op(G_S2 + 1, 4); }
        if (n == 16 && gemm) { m.op(G_S2 + 2, 4); m.op(G_S2 + 3, 4); }
        if (n == 17 && gemm) { m.op(G_S2 + 4, 4); m.op(G_S2 + 5, 4); }
    }
    m.point(P_C);
    m.op(G_FH + 2, 1); m.op(G_S2 + 6, 4);
    m.point(P_STEP - 1);
    for (int i = 0; i < P2_NS; ++i) {
        m.point(P_STEP + i);
        m.point(P_CHK2 + i);
        if (i + P2_SL < P2_NS) m.op(G_S2 + i + P2_SL, p2_ops(i + P2_SL));
        if (i == 7) m.op(G_FH + 3, 1);
        if (i == P2_POLLO) m.op(G_FO, 1);
        m.point(P_CHK2 + 100 + i);
        for (int st = 0; st < 8; ++st) if (p2_oissue(st) == i) m.op(G_O + st, 2);
    }
    return m.cnt;
}
constexpr int cmin(int a, int b) { return a < b ? a : b; }
constexpr int p1_wait(int n, bool gemm) { return ld_count(P_BAR + n, G_S1 + cmin(2 * n + 3, P1_NSUB - 1), gemm); }
constexpr int p2_wait(int i) {
    int c = ld_count(P_STEP + i, G_S2 + i + 1, true);
    if (i + 1 >= P2_ND && i + 1 < P2_NS) c = cmin(c, ld_count(P_STEP + i, G_O + (i + 1 - P2_ND), true));
    return i + 1 < P2_NS ? c : 0;
}

__global__ __launch_bounds__(768, 3) void fused_kernel(const u16* __restrict__ x, const u16* __restrict__ wqkv, const u16* __restrict__ wo, const u16* __restrict__ wg, const u16* __restrict__ wu,
                                                       const u16* __restrict__ wd, char* __restrict__ ws, u16* __restrict__ out) {
    __shared__ __attribute__((aligned(1024))) char lds_[163840];
    LAS char* lds = (LAS char*)lds_;
    const unsigned lds0 = (unsigned)(uintptr_t)lds;
    const int tid = threadIdx.x, lane = tid & 63, wid = __builtin_amdgcn_readfirstlane(tid >> 6);
    const int bid = blockIdx.x, xcd = bid & 7, idx = bid >> 3;
    const int cg = (xcd & 3) * 2 + (idx & 1), rt = (xcd >> 2) * 16 + (idx >> 1);
    const int g = lane >> 4;
    STAMP_DECL; STAMP(0);
    auto unit_kind = [&](int u) -> int { return u == 0 ? (cg >> 2) : (u == 1 && cg < 4) ? 2 : 3; };
    auto unit_idx = [&](int u) -> int { return u == 0 ? (cg & 3) : (cg < 4) ? (u == 1 ? cg : 3 * cg + u - 2) : 12 + 4 * (cg - 4) + u - 1; };

    if (wid >= 8) {
        const int w = wid - 8;
        const char* wsrc = nullptr; unsigned wstride = 0;
        auto set_unit_src = [&](int u) {
            const int kind = unit_kind(u), ui = unit_idx(u);
            if (kind == 3) { const int c32 = ((lane & 15) >> 1) ^ g; wstride = 3584; wsrc = (const char*)((c32 & 4) ? wu : wg) + ui * 128 + (c32 & 3) * 32 + (lane & 1) * 16; }
            else { const int ch = (((lane & 15) >> 1) ^ (g << 1)) * 2 + (lane & 1); wstride = 2688; wsrc = (const char*)wqkv + kind * 896 + ui * 224 + (ch < 13 ? ch : 13) * 16; }
            wsrc += (size_t)(16 * w + g) * wstride;
        };
        auto issue1 = [&](auto I) {
            constexpr int i = I; constexpr int u = i / 7, ks = i % 7, slot = (i + 4) % 10;
            if constexpr (ks == 0) set_unit_src(u);
#pragma unroll
            for (int j = 0; j < 4; ++j) glds16(wsrc + (size_t)(ks * 64 + 4 * j) * wstride, lds0 + slot * 16384 + w * 4096 + j * 1024);
        };
        u32x2 ev; ld8_sc1(ev, ws + WS_E);
        static_for<0, 6>(issue1);
        unsigned wm_[14];
#pragma unroll
        for (int u = 0; u < 5; ++u) {
            const int kind = unit_kind(u), ui = unit_idx(u);
            const int r = (rt & 15) * 28 + w * 7 + (lane >> 3 < 7 ? lane >> 3 : 6), c = lane & 3;
            const char* p = (kind == 3) ? (const char*)((c & 1) ? wu : wg) + (size_t)r * 3584 + ui * 128 : (const char*)wqkv + (size_t)r * 2688 + kind * 896 + ui * 224 + (c == 0 ? 0 : c == 1 ? 92 : c == 2 ? 128 : 220);
            asm volatile("global_load_dword %0, %1, off" : "=&v"(wm_[u]) : "v"(p) : "memory");
        }
#pragma unroll
        for (int k = 0; k < 2; ++k) {
            const int l = (idx * 4 + w) * 98 + k * 64 + lane; const int lc = (k * 64 + lane < 98) ? l : (idx * 4 + w) * 98;
            asm volatile("global_load_dword %0, %1, off" : "=&v"(wm_[5 + k]) : "v"((const char*)wd + (size_t)lc * 128) : "memory");
        }
        {
            const int l = (idx * 4 + w) * 25 + (lane < 25 ? lane : 0); const int lc = l < 3136 ? l : 0;
            asm volatile("global_load_dword %0, %1, off" : "=&v"(wm_[7]) : "v"((const char*)wo + (size_t)lc * 128) : "memory");
        }
        wait_vm_pin<32>(ev);
        const unsigned tl = __builtin_amdgcn_readfirstlane(ev[0]) + 1u, th = __builtin_amdgcn_readfirstlane(ev[1]) + (tl == 0u ? 1u : 0u);
        const u32x2 tag = {tl, th};
        if (w == 0 && lane == 0) st8_wt(ws + WS_FS + bid * 8, tag);
        const int head = xcd * 2 + (idx >> 4), qt = idx & 15;
        const bool gemm = idx < 28;
        const int ti = gemm ? xcd * 4 + idx / 7 : 0, cj = gemm ? idx % 7 : 0;
        const int wr = lane >> 3, wsl = lane & 7;
        const int wcb = cj * 128 + ((((wsl >> 1) ^ (wr & 2)) << 1) | (wsl & 1)) * 16;
        const char* wd_src = (const char*)wd + (size_t)(16 * w + wr) * 896 + wcb;
        const char* h_src = ws + WS_H + ((size_t)(ti * 2) * 224 + 2 * w) * 512 + lane * 16;
        const char* wo_src[2];
#pragma unroll
        for (int i = 0; i < 2; ++i) { const int d = (wr < 4 ? 0 : 12) + 4 * (2 * (w & 1) + i) + wr; wo_src[i] = (const char*)wo + (size_t)((w >> 1) * 28 + (d < 28 ? d : 27)) * 896 + wcb; }
        const unsigned ring = lds0 + w * 4096;
        auto issue2 = [&](auto I) {
            constexpr int i = I; constexpr int so = p2_slot(i) * 16384;
            if constexpr (i < P2_ND) {
                constexpr int u = p2_unit(i);
                glds16_sc1(h_src + (size_t)u * 4096, ring + so);
                glds16_sc1(h_src + (size_t)u * 4096 + 224 * 512, ring + so + 1024);
                glds16(wd_src + (size_t)u * 64 * 896, ring + so + 2048);
                glds16(wd_src + (size_t)(u * 64 + 8) * 896, ring + so + 3072);
            } else {
                glds16(wo_src[0] + (size_t)(i - P2_ND) * 56 * 896, ring + so + 2048);
                glds16(wo_src[1] + (size_t)(i - P2_ND) * 56 * 896, ring + so + 3072);
            }
        };
        auto issue_kv = [&](int j0, int j1) {
            for (int j = j0; j < j1; ++j) {
                const int k = 4 * j + w, lt = k < 24 ? k : k - 24; int at = qt * 8 - 16 + lt; at = at < 0 ? 0 : at;
                glds16_sc1(ws + (k < 24 ? WS_K : WS_V) + (size_t)(head * 128 + at) * 1024 + lane * 16, lds0 + k * 1024);
            }
        };
        int fu = 0;
        static_for<0, 28>([&](auto I) { if ((lane >> 1) == (int)I) fu = p2_unit(I); });
        const char* hflag = ws + WS_FH + ((size_t)((ti * 4 + w) * 32 + fu) * 2 + (lane & 1)) * 8;
        const char* oflag = ws + WS_FO + ((size_t)(ti * 2 + (w >> 1)) * 32 + (lane & 31)) * 8;
        const char* kflag;
        { const int li = lane < 12 ? lane : lane < 24 ? lane - 12 : 0; int r = 2 * qt - 4 + (li >> 1); r = r < 0 ? 0 : r;
          kflag = ws + WS_FQ + ((size_t)((head * 3 + (lane < 12 ? 1 : 2)) * 32 + r) * 2 + (li & 1)) * 8; }
        u32x2 fv, fq;
        raw_bar();
        raw_bar();
        asm volatile("s_waitcnt vmcnt(0)" : "+v"(wm_[0]), "+v"(wm_[1]), "+v"(wm_[2]), "+v"(wm_[3]), "+v"(wm_[4]), "+v"(wm_[5]), "+v"(wm_[6]), "+v"(wm_[7])::"memory");
        static_for<6, 10>(issue1);
        auto p1_loop = [&](auto GEMM) {
            constexpr bool GM = GEMM;
            static_for<0, P1_NINT>([&](auto N) {
                constexpr int n = N;
                wait_vm<p1_wait(n, GM)>();
                STAMP(20 + n);
                raw_bar();
                if constexpr (n == 13) {
                    if constexpr (GM) { wait_vm_pin<ld_count(P_CHK1 + 13, G_FH + 0, true)>(fv); pin1(fq); } else wait_vm_pin<ld_count(P_CHK1 + 13, G_FQ, false)>(fq);
                    poll_flags(fq, kflag, lane < 24, tag);
                    if constexpr (GM) poll_flags(fv, hflag, lane < 8, tag);
                }
                if constexpr (n == 17 && GM) { wait_vm_pin<ld_count(P_CHK1 + 17, G_FH + 1, true)>(fv); poll_flags(fv, hflag, lane < 24, tag); }
                if constexpr (n == 11) { ld8_sc1(fq, kflag); if constexpr (GM) { ld8_sc1(fv, hflag); } }
                if constexpr (n == 15 && GM) ld8_sc1(fv, hflag);
                if constexpr (n >= 1 && 2 * n + 8 < P1_NSUB) issue1(ic<(n >= 1 && 2 * n + 8 < P1_NSUB) ? 2 * n + 8 : 0>{});
                if constexpr (n >= 1 && 2 * n + 9 < P1_NSUB) issue1(ic<(n >= 1 && 2 * n + 9 < P1_NSUB) ? 2 * n + 9 : 0>{});
                if constexpr (n == 13 && GM) issue2(ic<0>{});
                if constexpr (n == 14) issue_kv(0, 8);
                if constexpr (n == 15) { issue_kv(8, 12); if constexpr (GM) issue2(ic<1>{}); }
                if constexpr (n == 16 && GM) { issue2(ic<2>{}); issue2(ic<3>{}); }
                if constexpr (n == 17 && GM) { issue2(ic<4>{}); issue2(ic<5>{}); }
            });
            wait_vm<ld_count(P_C, G_KV + 1, GM)>();
            raw_bar();
        };
        if (gemm) p1_loop(std::true_type{}); else p1_loop(std::false_type{});
        STAMP(1);
        if (!gemm) {
            if (bid == 255 && w == 0) {
                unsigned spins = 0;
                for (;;) {
                    bool ok = true;
#pragma unroll
                    for (int k = 0; k < 4; ++k) { u32x2 v; ld8_sc1_wait(v, ws + WS_FS + (k * 64 + lane) * 8); ok = ok && v[0] == tag[0] && v[1] == tag[1]; }
                    if (__all(ok) || ++spins > (1u << 18)) break;
                    __builtin_amdgcn_s_sleep(8);
                }
                if (lane == 0) st8_wt(ws + WS_E, tag);
            }
            return;
        }
        const int t32 = lane & 31, hh = lane >> 5, q4 = (lane & 15) >> 2, p4 = lane & 3;
        ld8_sc1(fv, hflag); issue2(ic<6>{});
        STAMP(2);
        f32x16 acc[2][2];
#pragma unroll
        for (int a = 0; a < 2; ++a)
#pragma unroll
            for (int b = 0; b < 2; ++b)
#pragma unroll
                for (int r = 0; r < 16; ++r) acc[a][b][r] = 0.f;
        const LAS char* rb = lds + w * 4096;
        unsigned wro[2];
#pragma unroll
        for (int ct = 0; ct < 2; ++ct) wro[ct] = 2048 + (8 * hh + q4) * 128 + (((2 * (ct ^ (w & 1)) + (g & 1)) ^ (q4 & 2)) << 5) + p4 * 8;
        const unsigned hro0 = (w >> 1) * 1024 + lane * 16, hro1 = (1 - (w >> 1)) * 1024 + lane * 16;
        bf16x8 wfr[2][2], hfr[2][2];
        u32x4 of[3][2];
        const char* op = ws + WS_O + ((size_t)(ti * 2 * 16 + (w >> 1)) * 4 + 2 * (w & 1)) * 512 + lane * 16;
        auto rd_stage = [&](auto I) {
            constexpr int i = I; constexpr int so = p2_slot(i) * 16384, b = i & 1;
            const LAS char* sb = rb + so;
#pragma unroll
            for (int ct = 0; ct < 2; ++ct) wfr[b][ct] = cat8(tr_read(sb + wro[ct]), tr_read(sb + wro[ct] + 512));
            if constexpr (i < P2_ND) { hfr[b][0] = *(const LAS bf16x8*)(sb + hro0); hfr[b][1] = *(const LAS bf16x8*)(sb + hro1); }
        };
        auto oload = [&](auto ST) { constexpr int st = ST; ld2_sc1(of[st % 3][0], of[st % 3][1], op + st * 4096 + (w >> 1) * 32768, op + st * 4096 + (1 - (w >> 1)) * 32768); };
        LAS f32x4* scr = (LAS f32x4*)lds;
        f32x16 gfin;
        auto reduce = [&](f32x16& res) {
#pragma unroll
            for (int d = 1; d < 4; ++d) {
                const f32x16& a = acc[d & 1][d >> 1];
#pragma unroll
                for (int k = 0; k < 4; ++k) scr[((w * 3 + d - 1) * 4 + k) * 64 + lane] = f32x4{a[4 * k], a[4 * k + 1], a[4 * k + 2], a[4 * k + 3]};
            }
            lds_bar();
            res = acc[0][0];
#pragma unroll
            for (int d = 1; d < 4; ++d)
#pragma unroll
                for (int k = 0; k < 4; ++k) { const f32x4 v = scr[(((w ^ d) * 3 + d - 1) * 4 + k) * 64 + lane]; res[4 * k] += v[0]; res[4 * k + 1] += v[1]; res[4 * k + 2] += v[2]; res[4 * k + 3] += v[3]; }
        };
        wait_vm<ld_count(P_STEP - 1, G_S2 + 0, true)>();
        rd_stage(ic<0>{});
        static_for<0, P2_NS>([&](auto I) {
            constexpr int i = I; constexpr int b = i & 1;
            asm volatile("s_waitcnt vmcnt(%0) lgkmcnt(0)" ::"n"(p2_wait(i)) : "memory");
            if constexpr (i == 5) { wait_vm_pin<ld_count(P_CHK2 + 5, G_FH + 2, true)>(fv); poll_flags(fv, hflag, lane < 40, tag); }
            if constexpr (i == 13) { wait_vm_pin<ld_count(P_CHK2 + 13, G_FH + 3, true)>(fv); poll_flags(fv, hflag, lane < 56, tag); }
            if constexpr (i + P2_SL < P2_NS) issue2(ic<(i + P2_SL < P2_NS ? i + P2_SL : 0)>{});
            if constexpr (i == 7) ld8_sc1(fv, hflag);
            if constexpr (i == P2_POLLO) ld8_sc1(fv, oflag);
            if constexpr (i == P2_OLD) {
                wait_vm_pin<ld_count(P_CHK2 + 100 + P2_OLD, G_FO, true)>(fv);
                poll_flags(fv, oflag, lane < 32, tag);
                raw_bar();
                oload(ic<0>{}); oload(ic<1>{}); oload(ic<2>{});
            }
            if constexpr (i + 1 < P2_NS) rd_stage(ic<(i + 1 < P2_NS ? i + 1 : 0)>{});
            if constexpr ((i & 3) == 0) { STAMP(10 + i / 4); }
            if constexpr (i < P2_ND) {
#pragma unroll
                for (int ct = 0; ct < 2; ++ct)
#pragma unroll
                    for (int tt = 0; tt < 2; ++tt) acc[ct][tt] = MFMA32(wfr[b][ct], hfr[b][tt], acc[ct][tt]);
                if constexpr (i == P2_ND - 1) {
                    reduce(gfin);
#pragma unroll
                    for (int a = 0; a < 2; ++a)
#pragma unroll
                        for (int c = 0; c < 2; ++c)
#pragma unroll
                            for (int r = 0; r < 16; ++r) acc[a][c][r] = 0.f;
                }
            } else {
                constexpr int st = i - P2_ND;
                pin2(of[st % 3][0], of[st % 3][1]);
#pragma unroll
                for (int ct = 0; ct < 2; ++ct)
#pragma unroll
                    for (int tt = 0; tt < 2; ++tt) acc[ct][tt] = MFMA32(wfr[b][ct], __builtin_bit_cast(bf16x8, of[st % 3][tt]), acc[ct][tt]);
                if constexpr (st + 3 < 8) oload(ic<(st + 3 < 8) ? st + 3 : 0>{});
            }
        });
        STAMP(7);
        const int tok = ti * 64 + (w >> 1) * 32 + t32, col0 = cj * 64 + (w & 1) * 32 + 8 * hh;
        const u32x4 xa = *(const GAS u32x4*)((const GAS char*)x + (size_t)tok * 896 + col0 * 2), xb = *(const GAS u32x4*)((const GAS char*)x + (size_t)tok * 896 + col0 * 2 + 32);
        lds_bar();
        f32x16 sfin;
        reduce(sfin);
        unsigned sp[8];
#pragma unroll
        for (int j = 0; j < 8; ++j) {
            float r2[2];
#pragma unroll
            for (int e = 0; e < 2; ++e) { const float f = rbf(gfin[2 * j + e]), gt = rbf(f * __builtin_amdgcn_rcpf(1.0f + __expf(-f))); r2[e] = rbf(sfin[2 * j + e]) * gt; }
            sp[j] = pk_bf16(r2[0], r2[1]);
        }
#pragma unroll
        for (int n = 0; n < 2; ++n) {
            const auto r0 = __builtin_amdgcn_permlane32_swap(sp[4 * n], sp[4 * n + 2], false, false), r1 = __builtin_amdgcn_permlane32_swap(sp[4 * n + 1], sp[4 * n + 3], false, false);
            const u32x4 sg = {r0[0], r1[0], r0[1], r1[1]}, xx = n ? xb : xa;
            u32x4 o;
#pragma unroll
            for (int j = 0; j < 4; ++j) o[j] = pk_bf16(lo_f(xx[j]) + lo_f(sg[j]), hi_f(xx[j]) + hi_f(sg[j]));
            *(GAS u32x4*)((GAS char*)out + (size_t)tok * 896 + col0 * 2 + n * 32) = o;
        }
        STAMP(8);
        if (w == 0) STAMP_END(1);
        return;
    }

    const int wm = wid >> 2, wn = wid & 3;
    const int t32 = lane & 31, hh = lane >> 5;
    u32x4 xv[7]; u32x2 ev; ld8_sc1(ev, ws + WS_E);
    {
        const char* xp = (const char*)x + (size_t)(rt * 64 + (tid >> 3)) * 896 + (tid & 7) * 16;
        asm volatile("global_load_dwordx4 %0, %7, off\n\tglobal_load_dwordx4 %1, %7, off offset:128\n\tglobal_load_dwordx4 %2, %7, off offset:256\n\tglobal_load_dwordx4 %3, %7, off offset:384\n\t"
                     "global_load_dwordx4 %4, %7, off offset:512\n\tglobal_load_dwordx4 %5, %7, off offset:640\n\tglobal_load_dwordx4 %6, %7, off offset:768"
                     : "=&v"(xv[0]), "=&v"(xv[1]), "=&v"(xv[2]), "=&v"(xv[3]), "=&v"(xv[4]), "=&v"(xv[5]), "=&v"(xv[6]) : "v"(xp) : "memory");
    }
    asm volatile("s_waitcnt vmcnt(0)" : "+v"(ev), "+v"(xv[0]), "+v"(xv[1]), "+v"(xv[2]), "+v"(xv[3]), "+v"(xv[4]), "+v"(xv[5]), "+v"(xv[6])::"memory");
    const unsigned tl_ = __builtin_amdgcn_readfirstlane(ev[0]) + 1u, th_ = __builtin_amdgcn_readfirstlane(ev[1]) + (tl_ == 0u ? 1u : 0u);
    const u32x2 tag = {tl_, th_};
    STAMP(1);
    {
        float v[56]; float s0 = 0.f, s1 = 0.f;
#pragma unroll
        for (int i = 0; i < 7; ++i)
#pragma unroll
            for (int j = 0; j < 4; ++j) { v[i * 8 + 2 * j] = lo_f(xv[i][j]); v[i * 8 + 2 * j + 1] = hi_f(xv[i][j]); s0 += v[i * 8 + 2 * j]; s1 += v[i * 8 + 2 * j + 1]; }
        const float mean = sum8(s0 + s1) * (1.0f / 448.f), mb = rbf(mean);
        float q0 = 0.f, q1 = 0.f;
#pragma unroll
        for (int i = 0; i < 28; ++i) { v[2 * i] -= mb; v[2 * i + 1] -= mb; q0 = __builtin_fmaf(v[2 * i], v[2 * i], q0); q1 = __builtin_fmaf(v[2 * i + 1], v[2 * i + 1], q1); }
        const float dm = mean - mb, var = __builtin_fmaf(-dm, dm, sum8(q0 + q1) * (1.0f / 448.f));
        const float den = rbf(__builtin_sqrtf(rbf(rbf(var) + 1e-5f))), rden = 1.0f / den;
        const int row = tid >> 3, c8 = tid & 7;
        LAS char* dst = lds + (((row >> 5) * 28 + (c8 >> 1)) * 64 + (c8 & 1) * 32 + ((row & 31) ^ c8)) * 16;
#pragma unroll
        for (int i = 0; i < 7; ++i) {
            u32x4 o;
#pragma unroll
            for (int j = 0; j < 4; ++j) { const unsigned dp = pk_bf16(v[i * 8 + 2 * j], v[i * 8 + 2 * j + 1]); o[j] = pk_bf16(lo_f(dp) * rden, hi_f(dp) * rden); }
            *(LAS u32x4*)(dst + i * 4096) = o;
        }
    }
    lds_bar();
    bf16x8 xf[28];
#pragma unroll
    for (int s = 0; s < 28; ++s) xf[s] = *(const LAS bf16x8*)(lds + ((wm * 28 + s) * 64 + hh * 32 + (t32 ^ ((2 * s + hh) & 7))) * 16);
    lds_bar();
    STAMP(2);
    {
        f32x16 acc;
        unsigned rdo[3];
        auto set_rd = [&](int u) {
            const int ln = opaque(lane), hh = ln >> 5, q4 = (ln & 15) >> 2, p4 = ln & 3, chh = (ln >> 4) & 1;
            const int kind = unit_kind(u);
            const int byte = (kind == 3) ? (chh * 4 + wn) * 32 + 8 * p4 : 56 * wn + 32 * chh + 8 * ((chh && p4 == 3) ? 2 : p4);
            const int sw = (kind == 3) ? q4 : (q4 << 1);
            rdo[0] = (unsigned)((8 * hh + q4) * 256 + ((((byte >> 5) ^ sw) << 5) | (byte & 31))); rdo[1] = rdo[0] + 65536u; rdo[2] = rdo[0] + 131072u;
        };
        bf16x8 wf[3];
        auto rd_frag = [&](auto FF) {
            constexpr int Fi = FF; constexpr int i = Fi / 4, kq = Fi % 4, slot = (i + 4) % 10, off = slot * 16384 + kq * 4096;
            if constexpr (Fi % 28 == 0) set_rd(i / 7);
            const LAS char* b = lds + rdo[off >> 16] + (off & 65535);
            wf[Fi % 3] = cat8(tr_read(b), tr_read(b + 1024));
        };
        auto flag_unit = [&](int u) {
            const int kind = unit_kind(u), ui = unit_idx(u);
            const int lane = opaque(tid) & 63;
            char* f = (kind == 3) ? ws + WS_FH + ((size_t)((rt * 4 + wn) * 32 + ui) * 2 + wm) * 8 : ws + WS_FQ + ((size_t)(((4 * ui + wn) * 3 + kind) * 32 + rt) * 2 + wm) * 8;
            if (lane == 0) st8_wt(f, tag);
        };
        auto epilogue = [&](auto U) {
            constexpr int u = U;
            const int kind = unit_kind(u), ui = unit_idx(u);
            const int ln = opaque(tid) & 63, t32 = ln & 31, hh = ln >> 5;
            if (kind == 3) {
                unsigned hp[4];
#pragma unroll
                for (int j = 0; j < 4; ++j) {
                    const unsigned gp = pk_bf16(acc[2 * j], acc[2 * j + 1]), up = pk_bf16(acc[8 + 2 * j], acc[8 + 2 * j + 1]);
                    hp[j] = pk_bf16(lo_f(gp) * lo_f(up), hi_f(gp) * hi_f(up));
                }
                const auto r0 = __builtin_amdgcn_permlane32_swap(hp[0], hp[2], false, false), r1 = __builtin_amdgcn_permlane32_swap(hp[1], hp[3], false, false);
                st16_wt(ws + WS_H + ((size_t)((rt * 2 + wm) * 224 + ui * 8 + wn * 2 + hh) * 32 + t32) * 16, u32x4{r0[0], r1[0], r0[1], r1[1]});
            } else {
                unsigned p[8];
#pragma unroll
                for (int j = 0; j < 8; ++j) p[j] = pk_bf16(acc[2 * j], acc[2 * j + 1]);
                if (hh) { p[6] = (kind == 2) ? 0x00003f80u : 0u; p[7] = 0u; }
                char* base = ws + (kind == 0 ? WS_Q : kind == 1 ? WS_K : WS_V) + (size_t)((4 * ui + wn) * 128 + rt * 4 + wm * 2 + (t32 >> 4)) * 1024;
                const int tt = (kind == 2) ? ((t32 & 15) ^ (8 * hh)) : (t32 & 15);
#pragma unroll
                for (int n = 0; n < 2; ++n) {
                    const auto r0 = __builtin_amdgcn_permlane32_swap(p[4 * n], p[4 * n + 2], false, false), r1 = __builtin_amdgcn_permlane32_swap(p[4 * n + 1], p[4 * n + 3], false, false);
                    st16_wt(base + ((2 * n + hh) * 16 + tt) * 16, u32x4{r0[0], r1[0], r0[1], r1[1]});
                }
            }
        };
        const int head = xcd * 2 + (idx >> 4), qt = idx & 15, a = wid;
        u32x2 fq; u32x4 qv;
        const int tq = qt * 8 + a;
        const char* qflag = ws + WS_FQ + ((size_t)((head * 3) * 32 + (tq >> 2)) * 2 + ((tq >> 1) & 1)) * 8;
        const char* qsrc = ws + WS_Q + (size_t)(head * 128 + tq) * 1024 + (opaque(tid) & 63) * 16;
        raw_bar();
        rd_frag(ic<0>{}); rd_frag(ic<1>{});
        static_for<0, 4 * P1_NSUB>([&](auto FF) {
            constexpr int Fi = FF;
            if constexpr (Fi % 8 == 0 && Fi > 0) { raw_bar(); if constexpr (Fi / 8 < 18) { STAMP(10 + Fi / 8); } }
            if constexpr (Fi + 2 < 4 * P1_NSUB) rd_frag(ic<(Fi + 2 < 4 * P1_NSUB ? Fi + 2 : 0)>{});
            if constexpr (Fi % 28 == 0) {
#pragma unroll
                for (int r = 0; r < 16; ++r) acc[r] = 0.f;
            }
            acc = MFMA32(wf[Fi % 3], xf[Fi % 28], acc);
            if constexpr (Fi % 28 == 27) epilogue(ic<Fi / 28>{});
            if constexpr (Fi % 28 == 7 && Fi >= 28) { wait_vm<0>(); flag_unit(Fi / 28 - 1); }
            if constexpr (Fi == 112 + 16) ld8_sc1(fq, qflag);
            if constexpr (Fi == 112 + 24) { wait_vm_pin<0>(fq); poll_flags(fq, qflag, true, tag); ld16_sc1(qv, qsrc); }
        });
        STAMP(3);
        asm volatile("s_waitcnt vmcnt(%1) lgkmcnt(0)\n\ts_barrier" : "+v"(qv) : "n"(1) : "memory");
        const int lane = opaque(tid) & 63, g = lane >> 4, q4 = (lane & 15) >> 2, p4 = lane & 3;
        constexpr unsigned LK = 0, LV = 24576;
        STAMP(4);
        const int t16 = lane & 15;
        const bf16x8 qf = __builtin_bit_cast(bf16x8, qv);
        f32x4 sc[17];
#pragma unroll
        for (int j = 0; j < 17; ++j) { const bf16x8 kf = *(const LAS bf16x8*)(lds + LK + (a + j) * 1024 + lane * 16); sc[j] = MFMA16(kf, qf, (f32x4{0.f, 0.f, 0.f, 0.f})); }
        SBAR(); asm volatile("s_nop 15"); SBAR();
        const float NEG = -1e30f, L2E = 1.4426950408889634f, SCALE = 0.18898223650461362f;
#pragma unroll
        for (int r = 0; r < 4; ++r) { if (4 * g + r <= t16) sc[0][r] = NEG; if (4 * g + r > t16) sc[16][r] = NEG; }
        if (qt < 2) {
            asm volatile("" ::: "memory");
            const int first = 16 - qt * 8 - a;
#pragma unroll
            for (int j = 0; j < 16; ++j) if (j < first) sc[j] = f32x4{NEG, NEG, NEG, NEG};
        }
        float mx = max3f(sc[0][0], sc[0][1], sc[0][2]);
        mx = max3f(mx, sc[0][3], sc[1][0]);
        mx = max3f(mx, sc[1][1], sc[1][2]);
#pragma unroll
        for (int j = 2; j < 16; ++j) { mx = max3f(mx, sc[j][0], sc[j][1]); mx = max3f(mx, sc[j][2], sc[j][3]); }
        mx = max3f(mx, sc[16][0], sc[16][1]); mx = max3f(mx, sc[16][2], sc[16][3]);
        mx = max2f_pad(mx, sc[1][3]);
        { const auto r = __builtin_amdgcn_permlane16_swap(__float_as_uint(mx), __float_as_uint(mx), false, false); mx = max2f_pad(__uint_as_float(r[0]), __uint_as_float(r[1])); }
        { const auto r = __builtin_amdgcn_permlane32_swap(__float_as_uint(mx), __float_as_uint(mx), false, false); mx = max2f_pad(__uint_as_float(r[0]), __uint_as_float(r[1])); }
        wait_vm<0>(); flag_unit(4);
        const float nm = -rbf(rbf(mx) * SCALE) * L2E;
        const f32x2 nm2 = {nm, nm}, l2 = {L2E, L2E}, sc2 = {SCALE, SCALE};
        u32x2 pp[18];
#pragma unroll
        for (int j = 0; j < 17; ++j) {
            unsigned pk[2];
#pragma unroll
            for (int r = 0; r < 2; ++r) {
                const unsigned p1 = pk_bf16(sc[j][2 * r], sc[j][2 * r + 1]);
                const f32x2 s1 = f32x2{lo_f(p1), hi_f(p1)} * sc2;
                const unsigned p2 = pk_bf16(s1[0], s1[1]);
                const f32x2 e = __builtin_elementwise_fma(f32x2{lo_f(p2), hi_f(p2)}, l2, nm2);
                pk[r] = pk_bf16(__builtin_amdgcn_exp2f(e[0]), __builtin_amdgcn_exp2f(e[1]));
            }
            pp[j] = u32x2{pk[0], pk[1]};
        }
        pp[17] = u32x2{0u, 0u};
        f32x4 o0 = {0.f, 0.f, 0.f, 0.f}, o1 = {0.f, 0.f, 0.f, 0.f};
        const int vkey = 4 * g + q4;
        const unsigned va0 = LV + a * 1024 + (((p4 >> 1) * 16 + (vkey ^ (8 * (p4 >> 1)))) * 16) + (p4 & 1) * 8;
#pragma unroll
        for (int jp = 0; jp < 9; ++jp) {
            const int ja = 2 * jp, jb = (jp < 8) ? 2 * jp + 1 : 2 * jp;
            const bf16x8 pf = __builtin_bit_cast(bf16x8, (u32x4{pp[2 * jp][0], pp[2 * jp][1], pp[2 * jp + 1][0], pp[2 * jp + 1][1]}));
            const bf16x8 v0 = cat8(tr_read(lds + va0 + ja * 1024), tr_read(lds + va0 + jb * 1024));
            const bf16x8 v1 = cat8(tr_read(lds + va0 + ja * 1024 + 512), tr_read(lds + va0 + jb * 1024 + 512));
            o0 = MFMA16(v0, pf, o0); o1 = MFMA16(v1, pf, o1);
        }
        const float l = __shfl(o1[0], 48 + t16), rl = 1.0f / l;
        u32x4 ov = {pk_bf16(o0[0] * rl, o0[1] * rl), pk_bf16(o0[2] * rl, o0[3] * rl), pk_bf16(o1[0] * rl, o1[1] * rl), pk_bf16(o1[2] * rl, o1[3] * rl)};
        if (g == 3) { ov[2] = 0u; ov[3] = 0u; }
        const int tl = qt * 8 + a;
        STAMP(5);
        st16_wt(ws + WS_O + ((size_t)((tl >> 1) * 16 + head) * 4 + g) * 512 + ((tl & 1) * 16 + t16) * 16, ov);
        wait_vm<0>();
        if (lane == 0) st8_wt(ws + WS_FO + ((size_t)(((tl >> 2) * 2 + (head & 1)) * 8 + (head >> 1)) * 4 + (tl & 3)) * 8, tag);
        STAMP(6);
        if (wid == 0) STAMP_END(0);
    }
}

extern "C" void kernel_launch(void* const* d_in, const int* in_sizes, int n_in, void* d_out, int out_size, void* d_ws, size_t ws_size, hipStream_t stream) {
    const u16* x = (const u16*)d_in[0]; const u16* wqkv = (const u16*)d_in[1]; const u16* wo = (const u16*)d_in[2];
    const u16* wg = (const u16*)d_in[4]; const u16* wu = (const u16*)d_in[5]; const u16* wd = (const u16*)d_in[6];
    fused_kernel<<<256, 768, 0, stream>>>(x, wqkv, wo, wg, wu, wd, (char*)d_ws, (u16*)d_out);
}
```

```cpp
#define WARM 1
#define CODE_LINES 360
#define CODE_BACK 0x4400
#define NO_PRIO 1
#include <hip/hip_runtime.h>
#include <stdint.h>
#include <type_traits>

typedef unsigned short u16;
typedef unsigned long long u64;
typedef short bf16x8 __attribute__((ext_vector_type(8)));
typedef short s16x4 __attribute__((ext_vector_type(4)));
typedef float f32x4 __attribute__((ext_vector_type(4)));
typedef float f32x16 __attribute__((ext_vector_type(16)));
typedef float f32x2 __attribute__((ext_vector_type(2)));
typedef unsigned u32x4 __attribute__((ext_vector_type(4)));
typedef unsigned u32x2 __attribute__((ext_vector_type(2)));
typedef __bf16 bf2 __attribute__((ext_vector_type(2)));
#define LAS __attribute__((address_space(3)))
#define GAS __attribute__((address_space(1)))
#define DI __device__ __forceinline__

constexpr size_t WS_E = 0;
constexpr size_t WS_FS = 4096;
constexpr size_t WS_FQ = 8192;
constexpr size_t WS_FH = 32768;
constexpr size_t WS_FO = 98304;
constexpr size_t WS_Q = 131072;
constexpr size_t WS_K = WS_Q + 2097152;
constexpr size_t WS_V = WS_K + 2097152;
constexpr size_t WS_H = WS_V + 2097152;
constexpr size_t WS_O = WS_H + 7340032;
constexpr size_t WS_END = WS_O + 2097152;

template <int N> using ic = std::integral_constant<int, N>;
template <int B, int E, class Fn> DI void static_for(Fn&& f) { if constexpr (B < E) { f(ic<B>{}); static_for<B + 1, E>(f); } }

DI unsigned pk_bf16(float a, float b) { f32x2 v = {a, b}; bf2 r = __builtin_convertvector(v, bf2); return __builtin_bit_cast(unsigned, r); }
DI float lo_f(unsigned p) { return __uint_as_float(p << 16); }
DI float hi_f(unsigned p) { return __uint_as_float(p & 0xffff0000u); }
DI float rbf(float a) { return lo_f(pk_bf16(a, 0.f)); }
#define MFMA16(a, b, c) __builtin_amdgcn_mfma_f32_16x16x32_bf16((a), (b), (c), 0, 0, 0)
#define MFMA32(a, b, c) __builtin_amdgcn_mfma_f32_32x32x16_bf16((a), (b), (c), 0, 0, 0)
#define SBAR() __builtin_amdgcn_sched_barrier(0)
DI void glds16(const void* g, unsigned lds_base) {
    unsigned sv;
    asm volatile("s_mov_b32 %0, m0\n\ts_mov_b32 m0, %2\n\ts_nop 0\n\tglobal_load_lds_dwordx4 %1, off\n\ts_mov_b32 m0, %0" : "=&s"(sv) : "v"(g), "s"(lds_base) : "memory");
}
DI void glds16_sc1(const void* g, unsigned lds_base) {
    unsigned sv;
    asm volatile("s_mov_b32 %0, m0\n\ts_mov_b32 m0, %2\n\ts_nop 0\n\tglobal_load_lds_dwordx4 %1, off sc1\n\ts_mov_b32 m0, %0" : "=&s"(sv) : "v"(g), "s"(lds_base) : "memory");
}
template <int N> DI void wait_bar() { asm volatile("s_waitcnt vmcnt(%0) lgkmcnt(0)\n\ts_barrier" ::"n"(N) : "memory"); }
template <int N> DI void wait_vm() { asm volatile("s_waitcnt vmcnt(%0)" ::"n"(N) : "memory"); }
DI void lds_bar() { asm volatile("s_waitcnt lgkmcnt(0)\n\ts_barrier" ::: "memory"); }
DI void raw_bar() { asm volatile("s_barrier" ::: "memory"); }
typedef short v4i16_t __attribute__((ext_vector_type(4)));
DI s16x4 tr_read(const LAS char* p) { return __builtin_bit_cast(s16x4, __builtin_amdgcn_ds_read_tr16_b64_v4i16((LAS v4i16_t*)p)); }
DI bf16x8 cat8(s16x4 a, s16x4 b) { return __builtin_shufflevector(a, b, 0, 1, 2, 3, 4, 5, 6, 7); }
DI void st16_wt(void* p, u32x4 v) { asm volatile("global_store_dwordx4 %0, %1, off sc1\n\ts_nop 1" ::"v"(p), "v"(v) : "memory"); }
DI void st8_wt(void* p, u32x2 v) { asm volatile("global_store_dwordx2 %0, %1, off sc1" ::"v"(p), "v"(v) : "memory"); }
DI void ld2_sc1(u32x4& a, u32x4& b, const char* p0, const char* p1) {
    asm volatile("global_load_dwordx4 %0, %2, off sc1\n\tglobal_load_dwordx4 %1, %3, off sc1" : "=&v"(a), "=&v"(b) : "v"(p0), "v"(p1) : "memory"); }
DI void ld16_sc1(u32x4& a, const char* p) { asm volatile("global_load_dwordx4 %0, %1, off sc1" : "=&v"(a) : "v"(p) : "memory"); }
DI void pin2(u32x4& a, u32x4& b) { asm volatile("" : "+v"(a), "+v"(b)::"memory"); }
DI void ld8_sc1(u32x2& v, const void* p) { asm volatile("global_load_dwordx2 %0, %1, off sc1" : "=&v"(v) : "v"(p) : "memory"); }
DI void ld8_sc1_wait(u32x2& v, const void* p) { asm volatile("global_load_dwordx2 %0, %1, off sc1\n\ts_waitcnt vmcnt(0)" : "=&v"(v) : "v"(p) : "memory"); }
DI void pin1(u32x2& v) { asm volatile("" : "+v"(v)::"memory"); }
template <int N> DI void wait_vm_pin(u32x2& v) { asm volatile("s_waitcnt vmcnt(%1)" : "+v"(v) : "n"(N) : "memory"); }
DI int opaque(int v) { asm volatile("" : "+v"(v)); return v; }
DI float max3f(float a, float b, float c) { float r; asm("v_max3_f32 %0, %1, %2, %3" : "=v"(r) : "v"(a), "v"(b), "v"(c)); return r; }
DI float max2f_pad(float a, float b) { float r; asm("v_max_f32 %0, %1, %2\n\ts_nop 1" : "=v"(r) : "v"(a), "v"(b)); return r; }
template <int CTRL> DI float dpp_add(float v) { return v + __builtin_bit_cast(float, __builtin_amdgcn_update_dpp(0, __builtin_bit_cast(int, v), CTRL, 0xf, 0xf, true)); }
DI float sum8(float v) { v = dpp_add<0xB1>(v); v = dpp_add<0x4E>(v); return dpp_add<0x141>(v); }
DI void poll_flags(u32x2& v, const void* p, bool use, u32x2 tag) {
    unsigned spins = 0;
    while (!__all(!use || (v[0] == tag[0] && v[1] == tag[1]))) {
        __builtin_amdgcn_s_sleep(2);
        ld8_sc1_wait(v, p);
        if (++spins > (1u << 18)) break;
    }
}

#define STAMP_DECL do { } while (0)
#define STAMP(n) do { } while (0)
#define STAMP_END(k) do { } while (0)

constexpr int P1_NSUB = 35, P1_NINT = 18;
constexpr int P2_ND = 28, P2_NS = 36, P2_SL = 7;
constexpr int p2_slot(int i) { constexpr int t[7] = {9, 3, 4, 5, 6, 7, 8}; return t[i % 7]; }
constexpr int p2_unit(int i) {
    constexpr int t[28] = {12, 16, 20, 24, 0, 3, 6, 9, 13, 17, 21, 25, 1, 4, 7, 10, 14, 18, 22, 26, 2, 5, 8, 11, 15, 19, 23, 27};
    return t[i];
}
constexpr int p2_ops(int s) { return s < P2_ND ? 4 : 2; }
constexpr int P2_POLLO = 19, P2_OLD = 24;
constexpr int p2_oissue(int st) { return st < 3 ? P2_OLD : P2_ND + st - 3; }
enum { G_S1 = 0  , G_S2 = 100  , G_KV = 200, G_FH = 300  , G_FQ = 310, G_FO = 311, G_O = 320  ,
       P_BAR = 1000  , P_CHK1 = 1100  , P_C = 1200, P_STEP = 1300  , P_CHK2 = 1400   };
struct LdSim { int pt, grp, cnt; bool seen, done;
    constexpr void op(int g, int n) { if (done) return; if (seen) cnt += n; if (g == grp) { seen = true; cnt = 0; } }
    constexpr void point(int p) { if (p == pt) done = true; } };
constexpr int ld_count(int pt, int grp, bool gemm) {
    LdSim m{pt, grp, 0, false, false};
    for (int i = 0; i < 10; ++i) m.op(G_S1 + i, 4);
    for (int n = 0; n < P1_NINT; ++n) {
        m.point(P_BAR + n);
        if (n == 13) m.point(P_CHK1 + 13);
        if (n == 17) m.point(P_CHK1 + 17);
        if (n == 11) { m.op(G_FQ, 1); if (gemm) m.op(G_FH + 0, 1); }
        if (n == 15 && gemm) m.op(G_FH + 1, 1);
        if (n >= 1) { if (2 * n + 8 < P1_NSUB) m.op(G_S1 + 2 * n + 8, 4); if (2 * n + 9 < P1_NSUB) m.op(G_S1 + 2 * n + 9, 4); }
        if (n == 13 && gemm) m.op(G_S2 + 0, 4);
        if (n == 14) m.op(G_KV + 0, 8);
        if (n == 15) { m.op(G_KV + 1, 4); if (gemm) m.op(G_S2 + 1, 4); }
        if (n == 16 && gemm) { m.op(G_S2 + 2, 4); m.op(G_S2 + 3, 4); }
        if (n == 17 && gemm) { m.op(G_S2 + 4, 4); m.op(G_S2 + 5, 4); }
    }
    m.point(P_C);
    m.op(G_FH + 2, 1); m.op(G_S2 + 6, 4);
    m.point(P_STEP - 1);
    for (int i = 0; i < P2_NS; ++i) {
        m.point(P_STEP + i);
        m.point(P_CHK2 + i);
        if (i + P2_SL < P2_NS) m.op(G_S2 + i + P2_SL, p2_ops(i + P2_SL));
        if (i == 7) m.op(G_FH + 3, 1);
        if (i == P2_POLLO) m.op(G_FO, 1);
        m.point(P_CHK2 + 100 + i);
        for (int st = 0; st < 8; ++st) if (p2_oissue(st) == i) m.op(G_O + st, 2);
    }
    return m.cnt;
}
constexpr int cmin(int a, int b) { return a < b ? a : b; }
constexpr int p1_wait(int n, bool gemm) { return ld_count(P_BAR + n, G_S1 + cmin(2 * n + 3, P1_NSUB - 1), gemm); }
constexpr int p2_wait(int i) {
    int c = ld_count(P_STEP + i, G_S2 + i + 1, true);
    if (i + 1 >= P2_ND && i + 1 < P2_NS) c = cmin(c, ld_count(P_STEP + i, G_O + (i + 1 - P2_ND), true));
    return i + 1 < P2_NS ? c : 0;
}

__global__ __launch_bounds__(768, 3) void fused_kernel(const u16* __restrict__ x, const u16* __restrict__ wqkv, const u16* __restrict__ wo, const u16* __restrict__ wg, const u16* __restrict__ wu,
                                                       const u16* __restrict__ wd, char* __restrict__ ws, u16* __restrict__ out) {
    __shared__ __attribute__((aligned(1024))) char lds_[163840];
    LAS char* lds = (LAS char*)lds_;
    const unsigned lds0 = (unsigned)(uintptr_t)lds;
    const int tid = threadIdx.x, lane = tid & 63, wid = __builtin_amdgcn_readfirstlane(tid >> 6);
    const int bid = blockIdx.x, xcd = bid & 7, idx = bid >> 3;
    const int cg = (xcd & 3) * 2 + (idx & 1), rt = (xcd >> 2) * 16 + (idx >> 1);
    const int g = lane >> 4;
    STAMP_DECL; STAMP(0);
    auto unit_kind = [&](int u) -> int { return u == 0 ? (cg >> 2) : (u == 1 && cg < 4) ? 2 : 3; };
    auto unit_idx = [&](int u) -> int { return u == 0 ? (cg & 3) : (cg < 4) ? (u == 1 ? cg : 3 * cg + u - 2) : 12 + 4 * (cg - 4) + u - 1; };

    if (wid >= 8) {
        const int w = wid - 8;
        const char* wsrc = nullptr; unsigned wstride = 0;
        auto set_unit_src = [&](int u) {
            const int kind = unit_kind(u), ui = unit_idx(u);
            if (kind == 3) { const int c32 = ((lane & 15) >> 1) ^ g; wstride = 3584; wsrc = (const char*)((c32 & 4) ? wu : wg) + ui * 128 + (c32 & 3) * 32 + (lane & 1) * 16; }
            else { const int ch = (((lane & 15) >> 1) ^ (g << 1)) * 2 + (lane & 1); wstride = 2688; wsrc = (const char*)wqkv + kind * 896 + ui * 224 + (ch < 13 ? ch : 13) * 16; }
            wsrc += (size_t)(16 * w + g) * wstride;
        };
        auto issue1 = [&](auto I) {
            constexpr int i = I; constexpr int u = i / 7, ks = i % 7, slot = (i + 4) % 10;
            if constexpr (ks == 0) set_unit_src(u);
#pragma unroll
            for (int j = 0; j < 4; ++j) glds16(wsrc + (size_t)(ks * 64 + 4 * j) * wstride, lds0 + slot * 16384 + w * 4096 + j * 1024);
        };
        unsigned cw_[2];
        {
            const char* pc = (const char*)__builtin_amdgcn_s_getpc();
            pc = (const char*)((uintptr_t)pc & ~(uintptr_t)127);
#pragma unroll
            for (int k = 0; k < 2; ++k) { const int l = (w * 2 + k) * 64 + lane; asm volatile("global_load_dword %0, %1, off" : "=&v"(cw_[k]) : "v"(pc + (size_t)(l < CODE_LINES ? l : 0) * 128 - CODE_BACK) : "memory"); }
        }
        u32x2 ev; ld8_sc1(ev, ws + WS_E);
        static_for<0, 6>(issue1);
        unsigned wm_[14];
#pragma unroll
        for (int u = 0; u < 5; ++u) {
            const int kind = unit_kind(u), ui = unit_idx(u);
            const int r = (rt & 15) * 28 + w * 7 + (lane >> 3 < 7 ? lane >> 3 : 6), c = lane & 3;
            const char* p = (kind == 3) ? (const char*)((c & 1) ? wu : wg) + (size_t)r * 3584 + ui * 128 : (const char*)wqkv + (size_t)r * 2688 + kind * 896 + ui * 224 + (c == 0 ? 0 : c == 1 ? 92 : c == 2 ? 128 : 220);
            asm volatile("global_load_dword %0, %1, off" : "=&v"(wm_[u]) : "v"(p) : "memory");
        }
#pragma unroll
        for (int k = 0; k < 2; ++k) {
            const int l = (idx * 4 + w) * 98 + k * 64 + lane; const int lc = (k * 64 + lane < 98) ? l : (idx * 4 + w) * 98;
            asm volatile("global_load_dword %0, %1, off" : "=&v"(wm_[5 + k]) : "v"((const char*)wd + (size_t)lc * 128) : "memory");
        }
        {
            const int l = (idx * 4 + w) * 25 + (lane < 25 ? lane : 0); const int lc = l < 3136 ? l : 0;
            asm volatile("global_load_dword %0, %1, off" : "=&v"(wm_[7]) : "v"((const char*)wo + (size_t)lc * 128) : "memory");
        }
        wait_vm_pin<32>(ev);
        const unsigned tl = __builtin_amdgcn_readfirstlane(ev[0]) + 1u, th = __builtin_amdgcn_readfirstlane(ev[1]) + (tl == 0u ? 1u : 0u);
        const u32x2 tag = {tl, th};
        if (w == 0 && lane == 0) st8_wt(ws + WS_FS + bid * 8, tag);
        const int head = xcd * 2 + (idx >> 4), qt = idx & 15;
        const bool gemm = idx < 28;
        const int ti = gemm ? xcd * 4 + idx / 7 : 0, cj = gemm ? idx % 7 : 0;
        const int wr = lane >> 3, wsl = lane & 7;
        const int wcb = cj * 128 + ((((wsl >> 1) ^ (wr & 2)) << 1) | (wsl & 1)) * 16;
        const char* wd_src = (const char*)wd + (size_t)(16 * w + wr) * 896 + wcb;
        const char* h_src = ws + WS_H + ((size_t)(ti * 2) * 224 + 2 * w) * 512 + lane * 16;
        const char* wo_src[2];
#pragma unroll
        for (int i = 0; i < 2; ++i) { const int d = (wr < 4 ? 0 : 12) + 4 * (2 * (w & 1) + i) + wr; wo_src[i] = (const char*)wo + (size_t)((w >> 1) * 28 + (d < 28 ? d : 27)) * 896 + wcb; }
        const unsigned ring = lds0 + w * 4096;
        auto issue2 = [&](auto I) {
            constexpr int i = I; constexpr int so = p2_slot(i) * 16384;
            if constexpr (i < P2_ND) {
                constexpr int u = p2_unit(i);
                glds16_sc1(h_src + (size_t)u * 4096, ring + so);
                glds16_sc1(h_src + (size_t)u * 4096 + 224 * 512, ring + so + 1024);
                glds16(wd_src + (size_t)u * 64 * 896, ring + so + 2048);
                glds16(wd_src + (size_t)(u * 64 + 8) * 896, ring + so + 3072);
            } else {
                glds16(wo_src[0] + (size_t)(i - P2_ND) * 56 * 896, ring + so + 2048);
                glds16(wo_src[1] + (size_t)(i - P2_ND) * 56 * 896, ring + so + 3072);
            }
        };
        auto issue_kv = [&](int j0, int j1) {
            for (int j = j0; j < j1; ++j) {
                const int k = 4 * j + w, lt = k < 24 ? k : k - 24; int at = qt * 8 - 16 + lt; at = at < 0 ? 0 : at;
                glds16_sc1(ws + (k < 24 ? WS_K : WS_V) + (size_t)(head * 128 + at) * 1024 + lane * 16, lds0 + k * 1024);
            }
        };
        int fu = 0;
        static_for<0, 28>([&](auto I) { if ((lane >> 1) == (int)I) fu = p2_unit(I); });
        const char* hflag = ws + WS_FH + ((size_t)((ti * 4 + w) * 32 + fu) * 2 + (lane & 1)) * 8;
        const char* oflag = ws + WS_FO + ((size_t)(ti * 2 + (w >> 1)) * 32 + (lane & 31)) * 8;
        const char* kflag;
        { const int li = lane < 12 ? lane : lane < 24 ? lane - 12 : 0; int r = 2 * qt - 4 + (li >> 1); r = r < 0 ? 0 : r;
          kflag = ws + WS_FQ + ((size_t)((head * 3 + (lane < 12 ? 1 : 2)) * 32 + r) * 2 + (li & 1)) * 8; }
        u32x2 fv, fq;
        raw_bar();
        raw_bar();
        asm volatile("s_waitcnt vmcnt(0)" : "+v"(wm_[0]), "+v"(wm_[1]), "+v"(wm_[2]), "+v"(wm_[3]), "+v"(wm_[4]), "+v"(wm_[5]), "+v"(wm_[6]), "+v"(wm_[7]), "+v"(cw_[0]), "+v"(cw_[1])::"memory");
        static_for<6, 10>(issue1);
        auto p1_loop = [&](auto GEMM) {
            constexpr bool GM = GEMM;
            static_for<0, P1_NINT>([&](auto N) {
                constexpr int n = N;
                wait_vm<p1_wait(n, GM)>();
                STAMP(20 + n);
                raw_bar();
                if constexpr (n == 13) {
                    if constexpr (GM) { wait_vm_pin<ld_count(P_CHK1 + 13, G_FH + 0, true)>(fv); pin1(fq); } else wait_vm_pin<ld_count(P_CHK1 + 13, G_FQ, false)>(fq);
                    poll_flags(fq, kflag, lane < 24, tag);
                    if constexpr (GM) poll_flags(fv, hflag, lane < 8, tag);
                }
                if constexpr (n == 17 && GM) { wait_vm_pin<ld_count(P_CHK1 + 17, G_FH + 1, true)>(fv); poll_flags(fv, hflag, lane < 24, tag); }
                if constexpr (n == 11) { ld8_sc1(fq, kflag); if constexpr (GM) { ld8_sc1(fv, hflag); } }
                if constexpr (n == 15 && GM) ld8_sc1(fv, hflag);
                if constexpr (n >= 1 && 2 * n + 8 < P1_NSUB) issue1(ic<(n >= 1 && 2 * n + 8 < P1_NSUB) ? 2 * n + 8 : 0>{});
                if constexpr (n >= 1 && 2 * n + 9 < P1_NSUB) issue1(ic<(n >= 1 && 2 * n + 9 < P1_NSUB) ? 2 * n + 9 : 0>{});
                if constexpr (n == 13 && GM) issue2(ic<0>{});
                if constexpr (n == 14) issue_kv(0, 8);
                if constexpr (n == 15) { issue_kv(8, 12); if constexpr (GM) issue2(ic<1>{}); }
                if constexpr (n == 16 && GM) { issue2(ic<2>{}); issue2(ic<3>{}); }
                if constexpr (n == 17 && GM) { issue2(ic<4>{}); issue2(ic<5>{}); }
            });
            wait_vm<ld_count(P_C, G_KV + 1, GM)>();
            raw_bar();
        };
        if (gemm) p1_loop(std::true_type{}); else p1_loop(std::false_type{});
        STAMP(1);
        if (!gemm) {
            if (bid == 255 && w == 0) {
                unsigned spins = 0;
                for (;;) {
                    bool ok = true;
#pragma unroll
                    for (int k = 0; k < 4; ++k) { u32x2 v; ld8_sc1_wait(v, ws + WS_FS + (k * 64 + lane) * 8); ok = ok && v[0] == tag[0] && v[1] == tag[1]; }
                    if (__all(ok) || ++spins > (1u << 18)) break;
                    __builtin_amdgcn_s_sleep(8);
                }
                if (lane == 0) st8_wt(ws + WS_E, tag);
            }
            return;
        }
        const int t32 = lane & 31, hh = lane >> 5, q4 = (lane & 15) >> 2, p4 = lane & 3;
        ld8_sc1(fv, hflag); issue2(ic<6>{});
        STAMP(2);
        f32x16 acc[2][2];
#pragma unroll
        for (int a = 0; a < 2; ++a)
#pragma unroll
            for (int b = 0; b < 2; ++b)
#pragma unroll
                for (int r = 0; r < 16; ++r) acc[a][b][r] = 0.f;
        const LAS char* rb = lds + w * 4096;
        unsigned wro[2];
#pragma unroll
        for (int ct = 0; ct < 2; ++ct) wro[ct] = 2048 + (8 * hh + q4) * 128 + (((2 * (ct ^ (w & 1)) + (g & 1)) ^ (q4 & 2)) << 5) + p4 * 8;
        const unsigned hro0 = (w >> 1) * 1024 + lane * 16, hro1 = (1 - (w >> 1)) * 1024 + lane * 16;
        bf16x8 wfr[2][2], hfr[2][2];
        u32x4 of[3][2];
        const char* op = ws + WS_O + ((size_t)(ti * 2 * 16 + (w >> 1)) * 4 + 2 * (w & 1)) * 512 + lane * 16;
        auto rd_stage = [&](auto I) {
            constexpr int i = I; constexpr int so = p2_slot(i) * 16384, b = i & 1;
            const LAS char* sb = rb + so;
#pragma unroll
            for (int ct = 0; ct < 2; ++ct) wfr[b][ct] = cat8(tr_read(sb + wro[ct]), tr_read(sb + wro[ct] + 512));
            if constexpr (i < P2_ND) { hfr[b][0] = *(const LAS bf16x8*)(sb + hro0); hfr[b][1] = *(const LAS bf16x8*)(sb + hro1); }
        };
        auto oload = [&](auto ST) { constexpr int st = ST; ld2_sc1(of[st % 3][0], of[st % 3][1], op + st * 4096 + (w >> 1) * 32768, op + st * 4096 + (1 - (w >> 1)) * 32768); };
        LAS f32x4* scr = (LAS f32x4*)lds;
        f32x16 gfin;
        auto reduce = [&](f32x16& res) {
#pragma unroll
            for (int d = 1; d < 4; ++d) {
                const f32x16& a = acc[d & 1][d >> 1];
#pragma unroll
                for (int k = 0; k < 4; ++k) scr[((w * 3 + d - 1) * 4 + k) * 64 + lane] = f32x4{a[4 * k], a[4 * k + 1], a[4 * k + 2], a[4 * k + 3]};
            }
            lds_bar();
            res = acc[0][0];
#pragma unroll
            for (int d = 1; d < 4; ++d)
#pragma unroll
                for (int k = 0; k < 4; ++k) { const f32x4 v = scr[(((w ^ d) * 3 + d - 1) * 4 + k) * 64 + lane]; res[4 * k] += v[0]; res[4 * k + 1] += v[1]; res[4 * k + 2] += v[2]; res[4 * k + 3] += v[3]; }
        };
        wait_vm<ld_count(P_STEP - 1, G_S2 + 0, true)>();
        rd_stage(ic<0>{});
        static_for<0, P2_NS>([&](auto I) {
            constexpr int i = I; constexpr int b = i & 1;
            asm volatile("s_waitcnt vmcnt(%0) lgkmcnt(0)" ::"n"(p2_wait(i)) : "memory");
            if constexpr (i == 5) { wait_vm_pin<ld_count(P_CHK2 + 5, G_FH + 2, true)>(fv); poll_flags(fv, hflag, lane < 40, tag); }
            if constexpr (i == 13) { wait_vm_pin<ld_count(P_CHK2 + 13, G_FH + 3, true)>(fv); poll_flags(fv, hflag, lane < 56, tag); }
            if constexpr (i + P2_SL < P2_NS) issue2(ic<(i + P2_SL < P2_NS ? i + P2_SL : 0)>{});
            if constexpr (i == 7) ld8_sc1(fv, hflag);
            if constexpr (i == P2_POLLO) ld8_sc1(fv, oflag);
            if constexpr (i == P2_OLD) {
                wait_vm_pin<ld_count(P_CHK2 + 100 + P2_OLD, G_FO, true)>(fv);
                poll_flags(fv, oflag, lane < 32, tag);
                raw_bar();
                oload(ic<0>{}); oload(ic<1>{}); oload(ic<2>{});
            }
            if constexpr (i + 1 < P2_NS) rd_stage(ic<(i + 1 < P2_NS ? i + 1 : 0)>{});
            if constexpr ((i & 3) == 0) { STAMP(10 + i / 4); }
            if constexpr (i < P2_ND) {
#pragma unroll
                for (int ct = 0; ct < 2; ++ct)
#pragma unroll
                    for (int tt = 0; tt < 2; ++tt) acc[ct][tt] = MFMA32(wfr[b][ct], hfr[b][tt], acc[ct][tt]);
                if constexpr (i == P2_ND - 1) {
                    reduce(gfin);
#pragma unroll
                    for (int a = 0; a < 2; ++a)
#pragma unroll
                        for (int c = 0; c < 2; ++c)
#pragma unroll
                            for (int r = 0; r < 16; ++r) acc[a][c][r] = 0.f;
                }
            } else {
                constexpr int st = i - P2_ND;
                pin2(of[st % 3][0], of[st % 3][1]);
#pragma unroll
                for (int ct = 0; ct < 2; ++ct)
#pragma unroll
                    for (int tt = 0; tt < 2; ++tt) acc[ct][tt] = MFMA32(wfr[b][ct], __builtin_bit_cast(bf16x8, of[st % 3][tt]), acc[ct][tt]);
                if constexpr (st + 3 < 8) oload(ic<(st + 3 < 8) ? st + 3 : 0>{});
            }
        });
        STAMP(7);
        const int tok = ti * 64 + (w >> 1) * 32 + t32, col0 = cj * 64 + (w & 1) * 32 + 8 * hh;
        const u32x4 xa = *(const GAS u32x4*)((const GAS char*)x + (size_t)tok * 896 + col0 * 2), xb = *(const GAS u32x4*)((const GAS char*)x + (size_t)tok * 896 + col0 * 2 + 32);
        lds_bar();
        f32x16 sfin;
        reduce(sfin);
        unsigned sp[8];
#pragma unroll
        for (int j = 0; j < 8; ++j) {
            float r2[2];
#pragma unroll
            for (int e = 0; e < 2; ++e) { const float f = rbf(gfin[2 * j + e]), gt = rbf(f * __builtin_amdgcn_rcpf(1.0f + __expf(-f))); r2[e] = rbf(sfin[2 * j + e]) * gt; }
            sp[j] = pk_bf16(r2[0], r2[1]);
        }
#pragma unroll
        for (int n = 0; n < 2; ++n) {
            const auto r0 = __builtin_amdgcn_permlane32_swap(sp[4 * n], sp[4 * n + 2], false, false), r1 = __builtin_amdgcn_permlane32_swap(sp[4 * n + 1], sp[4 * n + 3], false, false);
            const u32x4 sg = {r0[0], r1[0], r0[1], r1[1]}, xx = n ? xb : xa;
            u32x4 o;
#pragma unroll
            for (int j = 0; j < 4; ++j) o[j] = pk_bf16(lo_f(xx[j]) + lo_f(sg[j]), hi_f(xx[j]) + hi_f(sg[j]));
            *(GAS u32x4*)((GAS char*)out + (size_t)tok * 896 + col0 * 2 + n * 32) = o;
        }
        STAMP(8);
        if (w == 0) STAMP_END(1);
        return;
    }

    const int wm = wid >> 2, wn = wid & 3;
    const int t32 = lane & 31, hh = lane >> 5;
    u32x4 xv[7]; u32x2 ev; ld8_sc1(ev, ws + WS_E);
    {
        const char* xp = (const char*)x + (size_t)(rt * 64 + (tid >> 3)) * 896 + (tid & 7) * 16;
        asm volatile("global_load_dwordx4 %0, %7, off\n\tglobal_load_dwordx4 %1, %7, off offset:128\n\tglobal_load_dwordx4 %2, %7, off offset:256\n\tglobal_load_dwordx4 %3, %7, off offset:384\n\t"
                     "global_load_dwordx4 %4, %7, off offset:512\n\tglobal_load_dwordx4 %5, %7, off offset:640\n\tglobal_load_dwordx4 %6, %7, off offset:768"
                     : "=&v"(xv[0]), "=&v"(xv[1]), "=&v"(xv[2]), "=&v"(xv[3]), "=&v"(xv[4]), "=&v"(xv[5]), "=&v"(xv[6]) : "v"(xp) : "memory");
    }
    asm volatile("s_waitcnt vmcnt(0)" : "+v"(ev), "+v"(xv[0]), "+v"(xv[1]), "+v"(xv[2]), "+v"(xv[3]), "+v"(xv[4]), "+v"(xv[5]), "+v"(xv[6])::"memory");
    const unsigned tl_ = __builtin_amdgcn_readfirstlane(ev[0]) + 1u, th_ = __builtin_amdgcn_readfirstlane(ev[1]) + (tl_ == 0u ? 1u : 0u);
    const u32x2 tag = {tl_, th_};
    STAMP(1);
    {
        float v[56]; float s0 = 0.f, s1 = 0.f;
#pragma unroll
        for (int i = 0; i < 7; ++i)
#pragma unroll
            for (int j = 0; j < 4; ++j) { v[i * 8 + 2 * j] = lo_f(xv[i][j]); v[i * 8 + 2 * j + 1] = hi_f(xv[i][j]); s0 += v[i * 8 + 2 * j]; s1 += v[i * 8 + 2 * j + 1]; }
        const float mean = sum8(s0 + s1) * (1.0f / 448.f), mb = rbf(mean);
        float q0 = 0.f, q1 = 0.f;
#pragma unroll
        for (int i = 0; i < 28; ++i) { v[2 * i] -= mb; v[2 * i + 1] -= mb; q0 = __builtin_fmaf(v[2 * i], v[2 * i], q0); q1 = __builtin_fmaf(v[2 * i + 1], v[2 * i + 1], q1); }
        const float dm = mean - mb, var = __builtin_fmaf(-dm, dm, sum8(q0 + q1) * (1.0f / 448.f));
        const float den = rbf(__builtin_sqrtf(rbf(rbf(var) + 1e-5f))), rden = 1.0f / den;
        const int row = tid >> 3, c8 = tid & 7;
        LAS char* dst = lds + (((row >> 5) * 28 + (c8 >> 1)) * 64 + (c8 & 1) * 32 + ((row & 31) ^ c8)) * 16;
#pragma unroll
        for (int i = 0; i < 7; ++i) {
            u32x4 o;
#pragma unroll
            for (int j = 0; j < 4; ++j) { const unsigned dp = pk_bf16(v[i * 8 + 2 * j], v[i * 8 + 2 * j + 1]); o[j] = pk_bf16(lo_f(dp) * rden, hi_f(dp) * rden); }
            *(LAS u32x4*)(dst + i * 4096) = o;
        }
    }
    lds_bar();
    bf16x8 xf[28];
#pragma unroll
    for (int s = 0; s < 28; ++s) xf[s] = *(const LAS bf16x8*)(lds + ((wm * 28 + s) * 64 + hh * 32 + (t32 ^ ((2 * s + hh) & 7))) * 16);
    lds_bar();
    STAMP(2);
    {
        f32x16 acc;
        unsigned rdo[3];
        auto set_rd = [&](int u) {
            const int ln = opaque(lane), hh = ln >> 5, q4 = (ln & 15) >> 2, p4 = ln & 3, chh = (ln >> 4) & 1;
            const int kind = unit_kind(u);
            const int byte = (kind == 3) ? (chh * 4 + wn) * 32 + 8 * p4 : 56 * wn + 32 * chh + 8 * ((chh && p4 == 3) ? 2 : p4);
            const int sw = (kind == 3) ? q4 : (q4 << 1);
            rdo[0] = (unsigned)((8 * hh + q4) * 256 + ((((byte >> 5) ^ sw) << 5) | (byte & 31))); rdo[1] = rdo[0] + 65536u; rdo[2] = rdo[0] + 131072u;
        };
        bf16x8 wf[3];
        auto rd_frag = [&](auto FF) {
            constexpr int Fi = FF; constexpr int i = Fi / 4, kq = Fi % 4, slot = (i + 4) % 10, off = slot * 16384 + kq * 4096;
            if constexpr (Fi % 28 == 0) set_rd(i / 7);
            const LAS char* b = lds + rdo[off >> 16] + (off & 65535);
            wf[Fi % 3] = cat8(tr_read(b), tr_read(b + 1024));
        };
        auto flag_unit = [&](int u) {
            const int kind = unit_kind(u), ui = unit_idx(u);
            const int lane = opaque(tid) & 63;
            char* f = (kind == 3) ? ws + WS_FH + ((size_t)((rt * 4 + wn) * 32 + ui) * 2 + wm) * 8 : ws + WS_FQ + ((size_t)(((4 * ui + wn) * 3 + kind) * 32 + rt) * 2 + wm) * 8;
            if (lane == 0) st8_wt(f, tag);
        };
        auto epilogue = [&](auto U) {
            constexpr int u = U;
            const int kind = unit_kind(u), ui = unit_idx(u);
            const int ln = opaque(tid) & 63, t32 = ln & 31, hh = ln >> 5;
            if (kind == 3) {
                unsigned hp[4];
#pragma unroll
                for (int j = 0; j < 4; ++j) {
                    const unsigned gp = pk_bf16(acc[2 * j], acc[2 * j + 1]), up = pk_bf16(acc[8 + 2 * j], acc[8 + 2 * j + 1]);
                    hp[j] = pk_bf16(lo_f(gp) * lo_f(up), hi_f(gp) * hi_f(up));
                }
                const auto r0 = __builtin_amdgcn_permlane32_swap(hp[0], hp[2], false, false), r1 = __builtin_amdgcn_permlane32_swap(hp[1], hp[3], false, false);
                st16_wt(ws + WS_H + ((size_t)((rt * 2 + wm) * 224 + ui * 8 + wn * 2 + hh) * 32 + t32) * 16, u32x4{r0[0], r1[0], r0[1], r1[1]});
            } else {
                unsigned p[8];
#pragma unroll
                for (int j = 0; j < 8; ++j) p[j] = pk_bf16(acc[2 * j], acc[2 * j + 1]);
                if (hh) { p[6] = (kind == 2) ? 0x00003f80u : 0u; p[7] = 0u; }
                char* base = ws + (kind == 0 ? WS_Q : kind == 1 ? WS_K : WS_V) + (size_t)((4 * ui + wn) * 128 + rt * 4 + wm * 2 + (t32 >> 4)) * 1024;
                const int tt = (kind == 2) ? ((t32 & 15) ^ (8 * hh)) : (t32 & 15);
#pragma unroll
                for (int n = 0; n < 2; ++n) {
                    const auto r0 = __builtin_amdgcn_permlane32_swap(p[4 * n], p[4 * n + 2], false, false), r1 = __builtin_amdgcn_permlane32_swap(p[4 * n + 1], p[4 * n + 3], false, false);
                    st16_wt(base + ((2 * n + hh) * 16 + tt) * 16, u32x4{r0[0], r1[0], r0[1], r1[1]});
                }
            }
        };
        const int head = xcd * 2 + (idx >> 4), qt = idx & 15, a = wid;
        u32x2 fq; u32x4 qv;
        const int tq = qt * 8 + a;
        const char* qflag = ws + WS_FQ + ((size_t)((head * 3) * 32 + (tq >> 2)) * 2 + ((tq >> 1) & 1)) * 8;
        const char* qsrc = ws + WS_Q + (size_t)(head * 128 + tq) * 1024 + (opaque(tid) & 63) * 16;
        raw_bar();
        rd_frag(ic<0>{}); rd_frag(ic<1>{});
        static_for<0, 4 * P1_NSUB>([&](auto FF) {
            constexpr int Fi = FF;
            if constexpr (Fi % 8 == 0 && Fi > 0) { raw_bar(); if constexpr (Fi / 8 < 18) { STAMP(10 + Fi / 8); } }
            if constexpr (Fi + 2 < 4 * P1_NSUB) rd_frag(ic<(Fi + 2 < 4 * P1_NSUB ? Fi + 2 : 0)>{});
            if constexpr (Fi % 28 == 0) {
#pragma unroll
                for (int r = 0; r < 16; ++r) acc[r] = 0.f;
            }
            acc = MFMA32(wf[Fi % 3], xf[Fi % 28], acc);
            if constexpr (Fi % 28 == 27) epilogue(ic<Fi / 28>{});
            if constexpr (Fi % 28 == 7 && Fi >= 28) { wait_vm<0>(); flag_unit(Fi / 28 - 1); }
            if constexpr (Fi == 112 + 16) ld8_sc1(fq, qflag);
            if constexpr (Fi == 112 + 24) { wait_vm_pin<0>(fq); poll_flags(fq, qflag, true, tag); ld16_sc1(qv, qsrc); }
        });
        STAMP(3);
        asm volatile("s_waitcnt vmcnt(%1) lgkmcnt(0)\n\ts_barrier" : "+v"(qv) : "n"(1) : "memory");
        const int lane = opaque(tid) & 63, g = lane >> 4, q4 = (lane & 15) >> 2, p4 = lane & 3;
        constexpr unsigned LK = 0, LV = 24576;
        STAMP(4);
        const int t16 = lane & 15;
        const bf16x8 qf = __builtin_bit_cast(bf16x8, qv);
        f32x4 sc[17];
#pragma unroll
        for (int j = 0; j < 17; ++j) { const bf16x8 kf = *(const LAS bf16x8*)(lds + LK + (a + j) * 1024 + lane * 16); sc[j] = MFMA16(kf, qf, (f32x4{0.f, 0.f, 0.f, 0.f})); }
        SBAR(); asm volatile("s_nop 15"); SBAR();
        const float NEG = -1e30f, L2E = 1.4426950408889634f, SCALE = 0.18898223650461362f;
#pragma unroll
        for (int r = 0; r < 4; ++r) { if (4 * g + r <= t16) sc[0][r] = NEG; if (4 * g + r > t16) sc[16][r] = NEG; }
        if (qt < 2) {
            asm volatile("" ::: "memory");
            const int first = 16 - qt * 8 - a;
#pragma unroll
            for (int j = 0; j < 16; ++j) if (j < first) sc[j] = f32x4{NEG, NEG, NEG, NEG};
        }
        float mx = max3f(sc[0][0], sc[0][1], sc[0][2]);
        mx = max3f(mx, sc[0][3], sc[1][0]);
        mx = max3f(mx, sc[1][1], sc[1][2]);
#pragma unroll
        for (int j = 2; j < 16; ++j) { mx = max3f(mx, sc[j][0], sc[j][1]); mx = max3f(mx, sc[j][2], sc[j][3]); }
        mx = max3f(mx, sc[16][0], sc[16][1]); mx = max3f(mx, sc[16][2], sc[16][3]);
        mx = max2f_pad(mx, sc[1][3]);
        { const auto r = __builtin_amdgcn_permlane16_swap(__float_as_uint(mx), __float_as_uint(mx), false, false); mx = max2f_pad(__uint_as_float(r[0]), __uint_as_float(r[1])); }
        { const auto r = __builtin_amdgcn_permlane32_swap(__float_as_uint(mx), __float_as_uint(mx), false, false); mx = max2f_pad(__uint_as_float(r[0]), __uint_as_float(r[1])); }
        wait_vm<0>(); flag_unit(4);
        const float nm = -rbf(rbf(mx) * SCALE) * L2E;
        const f32x2 nm2 = {nm, nm}, l2 = {L2E, L2E}, sc2 = {SCALE, SCALE};
        u32x2 pp[18];
#pragma unroll
        for (int j = 0; j < 17; ++j) {
            unsigned pk[2];
#pragma unroll
            for (int r = 0; r < 2; ++r) {
                const unsigned p1 = pk_bf16(sc[j][2 * r], sc[j][2 * r + 1]);
                const f32x2 s1 = f32x2{lo_f(p1), hi_f(p1)} * sc2;
                const unsigned p2 = pk_bf16(s1[0], s1[1]);
                const f32x2 e = __builtin_elementwise_fma(f32x2{lo_f(p2), hi_f(p2)}, l2, nm2);
                pk[r] = pk_bf16(__builtin_amdgcn_exp2f(e[0]), __builtin_amdgcn_exp2f(e[1]));
            }
            pp[j] = u32x2{pk[0], pk[1]};
        }
        pp[17] = u32x2{0u, 0u};
        f32x4 o0 = {0.f, 0.f, 0.f, 0.f}, o1 = {0.f, 0.f, 0.f, 0.f};
        const int vkey = 4 * g + q4;
        const unsigned va0 = LV + a * 1024 + (((p4 >> 1) * 16 + (vkey ^ (8 * (p4 >> 1)))) * 16) + (p4 & 1) * 8;
#pragma unroll
        for (int jp = 0; jp < 9; ++jp) {
            const int ja = 2 * jp, jb = (jp < 8) ? 2 * jp + 1 : 2 * jp;
            const bf16x8 pf = __builtin_bit_cast(bf16x8, (u32x4{pp[2 * jp][0], pp[2 * jp][1], pp[2 * jp + 1][0], pp[2 * jp + 1][1]}));
            const bf16x8 v0 = cat8(tr_read(lds + va0 + ja * 1024), tr_read(lds + va0 + jb * 1024));
            const bf16x8 v1 = cat8(tr_read(lds + va0 + ja * 1024 + 512), tr_read(lds + va0 + jb * 1024 + 512));
            o0 = MFMA16(v0, pf, o0); o1 = MFMA16(v1, pf, o1);
        }
        const float l = __shfl(o1[0], 48 + t16), rl = 1.0f / l;
        u32x4 ov = {pk_bf16(o0[0] * rl, o0[1] * rl), pk_bf16(o0[2] * rl, o0[3] * rl), pk_bf16(o1[0] * rl, o1[1] * rl), pk_bf16(o1[2] * rl, o1[3] * rl)};
        if (g == 3) { ov[2] = 0u; ov[3] = 0u; }
        const int tl = qt * 8 + a;
        STAMP(5);
        st16_wt(ws + WS_O + ((size_t)((tl >> 1) * 16 + head) * 4 + g) * 512 + ((tl & 1) * 16 + t16) * 16, ov);
        wait_vm<0>();
        if (lane == 0) st8_wt(ws + WS_FO + ((size_t)(((tl >> 2) * 2 + (head & 1)) * 8 + (head >> 1)) * 4 + (tl & 3)) * 8, tag);
        STAMP(6);
        if (wid == 0) STAMP_END(0);
    }
}

extern "C" void kernel_launch(void* const* d_in, const int* in_sizes, int n_in, void* d_out, int out_size, void* d_ws, size_t ws_size, hipStream_t stream) {
    const u16* x = (const u16*)d_in[0]; const u16* wqkv = (const u16*)d_in[1]; const u16* wo = (const u16*)d_in[2];
    const u16* wg = (const u16*)d_in[4]; const u16* wu = (const u16*)d_in[5]; const u16* wd = (const u16*)d_in[6];
    fused_kernel<<<256, 768, 0, stream>>>(x, wqkv, wo, wg, wu, wd, (char*)d_ws, (u16*)d_out);
}
```

```cpp
#define WARM 1
#define NO_PRIO 1
#include <hip/hip_runtime.h>
#include <stdint.h>
#include <type_traits>

typedef unsigned short u16;
typedef unsigned long long u64;
typedef short bf16x8 __attribute__((ext_vector_type(8)));
typedef short s16x4 __attribute__((ext_vector_type(4)));
typedef float f32x4 __attribute__((ext_vector_type(4)));
typedef float f32x16 __attribute__((ext_vector_type(16)));
typedef float f32x2 __attribute__((ext_vector_type(2)));
typedef unsigned u32x4 __attribute__((ext_vector_type(4)));
typedef unsigned u32x2 __attribute__((ext_vector_type(2)));
typedef __bf16 bf2 __attribute__((ext_vector_type(2)));
#define LAS __attribute__((address_space(3)))
#define GAS __attribute__((address_space(1)))
#define DI __device__ __forceinline__

constexpr size_t WS_E = 0;
constexpr size_t WS_FS = 4096;
constexpr size_t WS_FQ = 8192;
constexpr size_t WS_FH = 32768;
constexpr size_t WS_FO = 98304;
constexpr size_t WS_Q = 131072;
constexpr size_t WS_K = WS_Q + 2097152;
constexpr size_t WS_V = WS_K + 2097152;
constexpr size_t WS_H = WS_V + 2097152;
constexpr size_t WS_O = WS_H + 7340032;
constexpr size_t WS_END = WS_O + 2097152;

template <int N> using ic = std::integral_constant<int, N>;
template <int B, int E, class Fn> DI void static_for(Fn&& f) { if constexpr (B < E) { f(ic<B>{}); static_for<B + 1, E>(f); } }

DI unsigned pk_bf16(float a, float b) { f32x2 v = {a, b}; bf2 r = __builtin_convertvector(v, bf2); return __builtin_bit_cast(unsigned, r); }
DI float lo_f(unsigned p) { return __uint_as_float(p << 16); }
DI float hi_f(unsigned p) { return __uint_as_float(p & 0xffff0000u); }
DI float rbf(float a) { return lo_f(pk_bf16(a, 0.f)); }
#define MFMA16(a, b, c) __builtin_amdgcn_mfma_f32_16x16x32_bf16((a), (b), (c), 0, 0, 0)
#define MFMA32(a, b, c) __builtin_amdgcn_mfma_f32_32x32x16_bf16((a), (b), (c), 0, 0, 0)
#define SBAR() __builtin_amdgcn_sched_barrier(0)
DI void glds16(const void* g, unsigned lds_base) {
    unsigned sv;
    asm volatile("s_mov_b32 %0, m0\n\ts_mov_b32 m0, %2\n\ts_nop 0\n\tglobal_load_lds_dwordx4 %1, off\n\ts_mov_b32 m0, %0" : "=&s"(sv) : "v"(g), "s"(lds_base) : "memory");
}
DI void glds16_sc1(const void* g, unsigned lds_base) {
    unsigned sv;
    asm volatile("s_mov_b32 %0, m0\n\ts_mov_b32 m0, %2\n\ts_nop 0\n\tglobal_load_lds_dwordx4 %1, off sc1\n\ts_mov_b32 m0, %0" : "=&s"(sv) : "v"(g), "s"(lds_base) : "memory");
}
typedef int i32x4 __attribute__((ext_vector_type(4)));
DI i32x4 make_srd(const void* p, unsigned bytes) { const uintptr_t a = (uintptr_t)p; return i32x4{(int)(unsigned)a, (int)((unsigned)(a >> 32) & 0xffffu), (int)bytes, 0x00020000}; }
DI void blds16(i32x4 srd, unsigned voff, unsigned soff, unsigned lds_base) {
    asm volatile("s_mov_b32 m0, %3\n\ts_nop 0\n\tbuffer_load_dwordx4 %0, %1, %2 offen lds" ::"v"(voff), "s"(srd), "s"(soff), "s"(lds_base) : "memory");
}
DI void blds16_sc1(i32x4 srd, unsigned voff, unsigned soff, unsigned lds_base) {
    asm volatile("s_mov_b32 m0, %3\n\ts_nop 0\n\tbuffer_load_dwordx4 %0, %1, %2 offen sc1 lds" ::"v"(voff), "s"(srd), "s"(soff), "s"(lds_base) : "memory");
}
template <int N> DI void wait_bar() { asm volatile("s_waitcnt vmcnt(%0) lgkmcnt(0)\n\ts_barrier" ::"n"(N) : "memory"); }
template <int N> DI void wait_vm() { asm volatile("s_waitcnt vmcnt(%0)" ::"n"(N) : "memory"); }
DI void lds_bar() { asm volatile("s_waitcnt lgkmcnt(0)\n\ts_barrier" ::: "memory"); }
DI void raw_bar() { asm volatile("s_barrier" ::: "memory"); }
typedef short v4i16_t __attribute__((ext_vector_type(4)));
DI s16x4 tr_read(const LAS char* p) { return __builtin_bit_cast(s16x4, __builtin_amdgcn_ds_read_tr16_b64_v4i16((LAS v4i16_t*)p)); }
DI bf16x8 cat8(s16x4 a, s16x4 b) { return __builtin_shufflevector(a, b, 0, 1, 2, 3, 4, 5, 6, 7); }
DI void st16_wt(void* p, u32x4 v) { asm volatile("global_store_dwordx4 %0, %1, off sc1\n\ts_nop 1" ::"v"(p), "v"(v) : "memory"); }
DI void st8_wt(void* p, u32x2 v) { asm volatile("global_store_dwordx2 %0, %1, off sc1" ::"v"(p), "v"(v) : "memory"); }
DI void ld2_sc1(u32x4& a, u32x4& b, const char* p0, const char* p1) {
    asm volatile("global_load_dwordx4 %0, %2, off sc1\n\tglobal_load_dwordx4 %1, %3, off sc1" : "=&v"(a), "=&v"(b) : "v"(p0), "v"(p1) : "memory"); }
DI void ld16_sc1(u32x4& a, const char* p) { asm volatile("global_load_dwordx4 %0, %1, off sc1" : "=&v"(a) : "v"(p) : "memory"); }
DI void pin2(u32x4& a, u32x4& b) { asm volatile("" : "+v"(a), "+v"(b)::"memory"); }
DI void ld8_sc1(u32x2& v, const void* p) { asm volatile("global_load_dwordx2 %0, %1, off sc1" : "=&v"(v) : "v"(p) : "memory"); }
DI void ld8_sc1_wait(u32x2& v, const void* p) { asm volatile("global_load_dwordx2 %0, %1, off sc1\n\ts_waitcnt vmcnt(0)" : "=&v"(v) : "v"(p) : "memory"); }
DI void pin8(unsigned (&v)[14]) { asm volatile("" : "+v"(v[0]), "+v"(v[1]), "+v"(v[2]), "+v"(v[3]), "+v"(v[4]), "+v"(v[5]), "+v"(v[6]), "+v"(v[7])::"memory"); }
DI void pin1(u32x2& v) { asm volatile("" : "+v"(v)::"memory"); }
template <int N> DI void wait_vm_pin(u32x2& v) { asm volatile("s_waitcnt vmcnt(%1)" : "+v"(v) : "n"(N) : "memory"); }
DI int opaque(int v) { asm volatile("" : "+v"(v)); return v; }
DI float max3f(float a, float b, float c) { float r; asm("v_max3_f32 %0, %1, %2, %3" : "=v"(r) : "v"(a), "v"(b), "v"(c)); return r; }
DI float max2f_pad(float a, float b) { float r; asm("v_max_f32 %0, %1, %2\n\ts_nop 1" : "=v"(r) : "v"(a), "v"(b)); return r; }
template <int CTRL> DI float dpp_add(float v) { return v + __builtin_bit_cast(float, __builtin_amdgcn_update_dpp(0, __builtin_bit_cast(int, v), CTRL, 0xf, 0xf, true)); }
DI float sum8(float v) { v = dpp_add<0xB1>(v); v = dpp_add<0x4E>(v); return dpp_add<0x141>(v); }
DI void poll_flags(u32x2& v, const void* p, bool use, u32x2 tag) {
    unsigned spins = 0;
    while (!__all(!use || (v[0] == tag[0] && v[1] == tag[1]))) {
        __builtin_amdgcn_s_sleep(2);
        ld8_sc1_wait(v, p);
        if (++spins > (1u << 18)) break;
    }
}

#define STAMP_DECL do { } while (0)
#define STAMP(n) do { } while (0)
#define STAMP_END(k) do { } while (0)

constexpr int P1_NSUB = 35, P1_NINT = 18;
constexpr int P2_ND = 28, P2_NS = 36, P2_SL = 7;
constexpr int p2_slot(int i) { constexpr int t[7] = {9, 3, 4, 5, 6, 7, 8}; return t[i % 7]; }
constexpr int p2_unit(int i) {
    constexpr int t[28] = {12, 16, 20, 24, 0, 3, 6, 9, 13, 17, 21, 25, 1, 4, 7, 10, 14, 18, 22, 26, 2, 5, 8, 11, 15, 19, 23, 27};
    return t[i];
}
constexpr int p2_ops(int s) { return s < P2_ND ? 4 : 2; }
constexpr int P2_POLLO = 19, P2_OLD = 24;
constexpr int p2_oissue(int st) { return st < 3 ? P2_OLD : P2_ND + st - 3; }
enum { G_S1 = 0  , G_S2 = 100  , G_KV = 200, G_FH = 300  , G_FQ = 310, G_FO = 311, G_O = 320  ,
       P_BAR = 1000  , P_CHK1 = 1100  , P_C = 1200, P_STEP = 1300  , P_CHK2 = 1400   };
struct LdSim { int pt, grp, cnt; bool seen, done;
    constexpr void op(int g, int n) { if (done) return; if (seen) cnt += n; if (g == grp) { seen = true; cnt = 0; } }
    constexpr void point(int p) { if (p == pt) done = true; } };
constexpr int ld_count(int pt, int grp, bool gemm) {
    LdSim m{pt, grp, 0, false, false};
    for (int i = 0; i < 6; ++i) m.op(G_S1 + i, 4);
    for (int n = 0; n < P1_NINT; ++n) {
        m.point(P_BAR + n);
        if (n == 13) m.point(P_CHK1 + 13);
        if (n == 17) m.point(P_CHK1 + 17);
        if (n == 0) for (int i = 6; i < 10; ++i) m.op(G_S1 + i, 4);
        if (n == 11) { m.op(G_FQ, 1); if (gemm) m.op(G_FH + 0, 1); }
        if (n == 15 && gemm) m.op(G_FH + 1, 1);
        if (n >= 1) { if (2 * n + 8 < P1_NSUB) m.op(G_S1 + 2 * n + 8, 4); if (2 * n + 9 < P1_NSUB) m.op(G_S1 + 2 * n + 9, 4); }
        if (n == 13 && gemm) m.op(G_S2 + 0, 4);
        if (n == 14) m.op(G_KV + 0, 8);
        if (n == 15) { m.op(G_KV + 1, 4); if (gemm) m.op(G_S2 + 1, 4); }
        if (n == 16 && gemm) { m.op(G_S2 + 2, 4); m.op(G_S2 + 3, 4); }
        if (n == 17 && gemm) { m.op(G_S2 + 4, 4); m.op(G_S2 + 5, 4); }
    }
    m.point(P_C);
    m.op(G_FH + 2, 1); m.op(G_S2 + 6, 4);
    m.point(P_STEP - 1);
    for (int i = 0; i < P2_NS; ++i) {
        m.point(P_STEP + i);
        m.point(P_CHK2 + i);
        if (i + P2_SL < P2_NS) m.op(G_S2 + i + P2_SL, p2_ops(i + P2_SL));
        if (i == 7) m.op(G_FH + 3, 1);
        if (i == P2_POLLO) m.op(G_FO, 1);
        m.point(P_CHK2 + 100 + i);
        for (int st = 0; st < 8; ++st) if (p2_oissue(st) == i) m.op(G_O + st, 2);
    }
    return m.cnt;
}
constexpr int cmin(int a, int b) { return a < b ? a : b; }
constexpr int p1_wait(int n, bool gemm) { return ld_count(P_BAR + n, G_S1 + cmin(2 * n + 3, P1_NSUB - 1), gemm); }
constexpr int p2_wait(int i) {
    int c = ld_count(P_STEP + i, G_S2 + i + 1, true);
    if (i + 1 >= P2_ND && i + 1 < P2_NS) c = cmin(c, ld_count(P_STEP + i, G_O + (i + 1 - P2_ND), true));
    return i + 1 < P2_NS ? c : 0;
}

__global__ __launch_bounds__(768, 3) void fused_kernel(const u16* __restrict__ x, const u16* __restrict__ wqkv, const u16* __restrict__ wo, const u16* __restrict__ wg, const u16* __restrict__ wu,
                                                       const u16* __restrict__ wd, char* __restrict__ ws, u16* __restrict__ out) {
    __shared__ __attribute__((aligned(1024))) char lds_[163840];
    LAS char* lds = (LAS char*)lds_;
    const unsigned lds0 = (unsigned)(uintptr_t)lds;
    const int tid = threadIdx.x, lane = tid & 63, wid = __builtin_amdgcn_readfirstlane(tid >> 6);
    const int bid = blockIdx.x, xcd = bid & 7, idx = bid >> 3;
    const int cg = (xcd & 3) * 2 + (idx & 1), rt = (xcd >> 2) * 16 + (idx >> 1);
    const int g = lane >> 4;
    STAMP_DECL; STAMP(0);
    auto unit_kind = [&](int u) -> int { return u == 0 ? (cg >> 2) : (u == 1 && cg < 4) ? 2 : 3; };
    auto unit_idx = [&](int u) -> int { return u == 0 ? (cg & 3) : (cg < 4) ? (u == 1 ? cg : 3 * cg + u - 2) : 12 + 4 * (cg - 4) + u - 1; };

    if (wid >= 8) {
        const int w = wid - 8;
        const i32x4 srd_g = make_srd(wg, 448 * 3584), srd_u = make_srd(wu, 448 * 3584), srd_q = make_srd(wqkv, 448 * 2688), srd_ws = make_srd(ws, (unsigned)WS_END);
        const int lr = lane >> 3, ls = lane & 7;
        unsigned vg[2], vq[2];
#pragma unroll
        for (int rg = 0; rg < 2; ++rg) {
            vg[rg] = (unsigned)(lr * 3584 + (((ls >> 1) ^ (((lr >> 1) & 1) | (rg << 1))) * 32) + (ls & 1) * 16);
            const int ch = rg * 8 + ((ls >> 1) ^ (lr & 2)) * 2 + (ls & 1);
            vq[rg] = (unsigned)(lr * 2688 + (ch < 13 ? ch : 13) * 16);
        }
        auto issue1 = [&](auto I) {
            constexpr int i = I; constexpr int u = i / 7, ks = i % 7, slot = (i + 4) % 10;
            const int kind = unit_kind(u), ui = unit_idx(u);
            const unsigned dst = lds0 + slot * 16384 + w * 2048;
            if (kind == 3) {
                const unsigned so = (unsigned)((ks * 64 + 16 * w) * 3584 + ui * 128);
                blds16(srd_g, vg[0], so, dst); blds16(srd_g, vg[0], so + 8 * 3584, dst + 1024);
                blds16(srd_u, vg[1], so, dst + 8192); blds16(srd_u, vg[1], so + 8 * 3584, dst + 8192 + 1024);
            } else {
                const unsigned so = (unsigned)((ks * 64 + 16 * w) * 2688 + kind * 896 + ui * 224);
                blds16(srd_q, vq[0], so, dst); blds16(srd_q, vq[0], so + 8 * 2688, dst + 1024);
                blds16(srd_q, vq[1], so, dst + 8192); blds16(srd_q, vq[1], so + 8 * 2688, dst + 8192 + 1024);
            }
        };
        u32x2 ev; ld8_sc1(ev, ws + WS_E);
        static_for<0, 6>(issue1);
        unsigned wm_[14];
#pragma unroll
        for (int u = 0; u < 5; ++u) {
            const int kind = unit_kind(u), ui = unit_idx(u);
            const int r = (rt & 15) * 28 + w * 7 + (lane >> 3 < 7 ? lane >> 3 : 6), c = lane & 3;
            const char* p = (kind == 3) ? (const char*)((c & 1) ? wu : wg) + (size_t)r * 3584 + ui * 128 : (const char*)wqkv + (size_t)r * 2688 + kind * 896 + ui * 224 + (c == 0 ? 0 : c == 1 ? 92 : c == 2 ? 128 : 220);
            asm volatile("global_load_dword %0, %1, off" : "=&v"(wm_[u]) : "v"(p) : "memory");
        }
#pragma unroll
        for (int k = 0; k < 2; ++k) {
            const int l = (idx * 4 + w) * 98 + k * 64 + lane; const int lc = (k * 64 + lane < 98) ? l : (idx * 4 + w) * 98;
            asm volatile("global_load_dword %0, %1, off" : "=&v"(wm_[5 + k]) : "v"((const char*)wd + (size_t)lc * 128) : "memory");
        }
        {
            const int l = (idx * 4 + w) * 25 + (lane < 25 ? lane : 0); const int lc = l < 3136 ? l : 0;
            asm volatile("global_load_dword %0, %1, off" : "=&v"(wm_[7]) : "v"((const char*)wo + (size_t)lc * 128) : "memory");
        }
        wait_vm_pin<32>(ev);
        const unsigned tl = __builtin_amdgcn_readfirstlane(ev[0]) + 1u, th = __builtin_amdgcn_readfirstlane(ev[1]) + (tl == 0u ? 1u : 0u);
        const u32x2 tag = {tl, th};
        if (w == 0 && lane == 0) st8_wt(ws + WS_FS + bid * 8, tag);
        const int head = xcd * 2 + (idx >> 4), qt = idx & 15;
        const bool gemm = idx < 28;
        const int ti = gemm ? xcd * 4 + idx / 7 : 0, cj = gemm ? idx % 7 : 0;
        const int wr = lane >> 3, wsl = lane & 7;
        const int wcb = cj * 128 + ((((wsl >> 1) ^ (wr & 2)) << 1) | (wsl & 1)) * 16;
        const i32x4 srd_d = make_srd(wd, 1792 * 896), srd_o = make_srd(wo, 448 * 896);
        const unsigned vd = (unsigned)(wr * 896 + wcb), vl = (unsigned)(lane * 16);
        unsigned vo[2];
#pragma unroll
        for (int i = 0; i < 2; ++i) { const int d = (wr < 4 ? 0 : 12) + 4 * (2 * (w & 1) + i) + wr; vo[i] = (unsigned)(((w >> 1) * 28 + (d < 28 ? d : 27)) * 896 + wcb); }
        const unsigned hso = (unsigned)(WS_H + ((size_t)(ti * 2) * 224 + 2 * w) * 512);
        const unsigned ring = lds0 + w * 4096;
        auto issue2 = [&](auto I) {
            constexpr int i = I; constexpr int so = p2_slot(i) * 16384;
            if constexpr (i < P2_ND) {
                constexpr int u = p2_unit(i);
                blds16_sc1(srd_ws, vl, hso + u * 4096, ring + so);
                blds16_sc1(srd_ws, vl, hso + u * 4096 + 224 * 512, ring + so + 1024);
                blds16(srd_d, vd, (unsigned)((u * 64 + 16 * w) * 896), ring + so + 2048);
                blds16(srd_d, vd, (unsigned)((u * 64 + 16 * w + 8) * 896), ring + so + 3072);
            } else {
                blds16(srd_o, vo[0], (unsigned)((i - P2_ND) * 56 * 896), ring + so + 2048);
                blds16(srd_o, vo[1], (unsigned)((i - P2_ND) * 56 * 896), ring + so + 3072);
            }
        };
        auto issue_kv = [&](int j0, int j1) {
#pragma unroll
            for (int j = j0; j < j1; ++j) {
                const int k = 4 * j + w, lt = k < 24 ? k : k - 24; int at = qt * 8 - 16 + lt; at = at < 0 ? 0 : at;
                blds16_sc1(srd_ws, vl, (unsigned)((k < 24 ? WS_K : WS_V) + (size_t)(head * 128 + at) * 1024), lds0 + k * 1024);
            }
        };
        int fu = 0;
        static_for<0, 28>([&](auto I) { if ((lane >> 1) == (int)I) fu = p2_unit(I); });
        const char* hflag = ws + WS_FH + ((size_t)((ti * 4 + w) * 32 + fu) * 2 + (lane & 1)) * 8;
        const char* oflag = ws + WS_FO + ((size_t)(ti * 2 + (w >> 1)) * 32 + (lane & 31)) * 8;
        const char* kflag;
        { const int li = lane < 12 ? lane : lane < 24 ? lane - 12 : 0; int r = 2 * qt - 4 + (li >> 1); r = r < 0 ? 0 : r;
          kflag = ws + WS_FQ + ((size_t)((head * 3 + (lane < 12 ? 1 : 2)) * 32 + r) * 2 + (li & 1)) * 8; }
        u32x2 fv, fq;
        raw_bar();
        raw_bar();
        auto p1_loop = [&](auto GEMM) {
            constexpr bool GM = GEMM;
            static_for<0, P1_NINT>([&](auto N) {
                constexpr int n = N;
                wait_vm<p1_wait(n, GM)>();
                if constexpr (n == 2) pin8(wm_);
                STAMP(20 + n);
                raw_bar();
                if constexpr (n == 13) {
                    if constexpr (GM) { wait_vm_pin<ld_count(P_CHK1 + 13, G_FH + 0, true)>(fv); pin1(fq); } else wait_vm_pin<ld_count(P_CHK1 + 13, G_FQ, false)>(fq);
                    poll_flags(fq, kflag, lane < 24, tag);
                    if constexpr (GM) poll_flags(fv, hflag, lane < 8, tag);
                }
                if constexpr (n == 17 && GM) { wait_vm_pin<ld_count(P_CHK1 + 17, G_FH + 1, true)>(fv); poll_flags(fv, hflag, lane < 24, tag); }
                if constexpr (n == 0) static_for<6, 10>(issue1);
                if constexpr (n == 11) { ld8_sc1(fq, kflag); if constexpr (GM) { ld8_sc1(fv, hflag); } }
                if constexpr (n == 15 && GM) ld8_sc1(fv, hflag);
                if constexpr (n >= 1 && 2 * n + 8 < P1_NSUB) issue1(ic<(n >= 1 && 2 * n + 8 < P1_NSUB) ? 2 * n + 8 : 0>{});
                if constexpr (n >= 1 && 2 * n + 9 < P1_NSUB) issue1(ic<(n >= 1 && 2 * n + 9 < P1_NSUB) ? 2 * n + 9 : 0>{});
                if constexpr (n == 13 && GM) issue2(ic<0>{});
                if constexpr (n == 14) issue_kv(0, 8);
                if constexpr (n == 15) { issue_kv(8, 12); if constexpr (GM) issue2(ic<1>{}); }
                if constexpr (n == 16 && GM) { issue2(ic<2>{}); issue2(ic<3>{}); }
                if constexpr (n == 17 && GM) { issue2(ic<4>{}); issue2(ic<5>{}); }
            });
            wait_vm<ld_count(P_C, G_KV + 1, GM)>();
            raw_bar();
        };
        if (gemm) p1_loop(std::true_type{}); else p1_loop(std::false_type{});
        STAMP(1);
        if (!gemm) {
            if (bid == 255 && w == 0) {
                unsigned spins = 0;
                for (;;) {
                    bool ok = true;
#pragma unroll
                    for (int k = 0; k < 4; ++k) { u32x2 v; ld8_sc1_wait(v, ws + WS_FS + (k * 64 + lane) * 8); ok = ok && v[0] == tag[0] && v[1] == tag[1]; }
                    if (__all(ok) || ++spins > (1u << 18)) break;
                    __builtin_amdgcn_s_sleep(8);
                }
                if (lane == 0) st8_wt(ws + WS_E, tag);
            }
            return;
        }
        const int t32 = lane & 31, hh = lane >> 5, q4 = (lane & 15) >> 2, p4 = lane & 3;
        ld8_sc1(fv, hflag); issue2(ic<6>{});
        STAMP(2);
        f32x16 acc[2][2];
#pragma unroll
        for (int a = 0; a < 2; ++a)
#pragma unroll
            for (int b = 0; b < 2; ++b)
#pragma unroll
                for (int r = 0; r < 16; ++r) acc[a][b][r] = 0.f;
        const LAS char* rb = lds + w * 4096;
        unsigned wro[2];
#pragma unroll
        for (int ct = 0; ct < 2; ++ct) wro[ct] = 2048 + (8 * hh + q4) * 128 + (((2 * (ct ^ (w & 1)) + (g & 1)) ^ (q4 & 2)) << 5) + p4 * 8;
        const unsigned hro0 = (w >> 1) * 1024 + lane * 16, hro1 = (1 - (w >> 1)) * 1024 + lane * 16;
        bf16x8 wfr[2][2], hfr[2][2];
        u32x4 of[3][2];
        const char* op = ws + WS_O + ((size_t)(ti * 2 * 16 + (w >> 1)) * 4 + 2 * (w & 1)) * 512 + lane * 16;
        auto rd_stage = [&](auto I) {
            constexpr int i = I; constexpr int so = p2_slot(i) * 16384, b = i & 1;
            const LAS char* sb = rb + so;
#pragma unroll
            for (int ct = 0; ct < 2; ++ct) wfr[b][ct] = cat8(tr_read(sb + wro[ct]), tr_read(sb + wro[ct] + 512));
            if constexpr (i < P2_ND) { hfr[b][0] = *(const LAS bf16x8*)(sb + hro0); hfr[b][1] = *(const LAS bf16x8*)(sb + hro1); }
        };
        auto oload = [&](auto ST) { constexpr int st = ST; ld2_sc1(of[st % 3][0], of[st % 3][1], op + st * 4096 + (w >> 1) * 32768, op + st * 4096 + (1 - (w >> 1)) * 32768); };
        LAS f32x4* scr = (LAS f32x4*)lds;
        f32x16 gfin;
        auto reduce = [&](f32x16& res) {
#pragma unroll
            for (int d = 1; d < 4; ++d) {
                const f32x16& a = acc[d & 1][d >> 1];
#pragma unroll
                for (int k = 0; k < 4; ++k) scr[((w * 3 + d - 1) * 4 + k) * 64 + lane] = f32x4{a[4 * k], a[4 * k + 1], a[4 * k + 2], a[4 * k + 3]};
            }
            lds_bar();
            res = acc[0][0];
#pragma unroll
            for (int d = 1; d < 4; ++d)
#pragma unroll
                for (int k = 0; k < 4; ++k) { const f32x4 v = scr[(((w ^ d) * 3 + d - 1) * 4 + k) * 64 + lane]; res[4 * k] += v[0]; res[4 * k + 1] += v[1]; res[4 * k + 2] += v[2]; res[4 * k + 3] += v[3]; }
        };
        wait_vm<ld_count(P_STEP - 1, G_S2 + 0, true)>();
        rd_stage(ic<0>{});
        static_for<0, P2_NS>([&](auto I) {
            constexpr int i = I; constexpr int b = i & 1;
            asm volatile("s_waitcnt vmcnt(%0) lgkmcnt(0)" ::"n"(p2_wait(i)) : "memory");
            if constexpr (i == 5) { wait_vm_pin<ld_count(P_CHK2 + 5, G_FH + 2, true)>(fv); poll_flags(fv, hflag, lane < 40, tag); }
            if constexpr (i == 13) { wait_vm_pin<ld_count(P_CHK2 + 13, G_FH + 3, true)>(fv); poll_flags(fv, hflag, lane < 56, tag); }
            if constexpr (i + P2_SL < P2_NS) issue2(ic<(i + P2_SL < P2_NS ? i + P2_SL : 0)>{});
            if constexpr (i == 7) ld8_sc1(fv, hflag);
            if constexpr (i == P2_POLLO) ld8_sc1(fv, oflag);
            if constexpr (i == P2_OLD) {
                wait_vm_pin<ld_count(P_CHK2 + 100 + P2_OLD, G_FO, true)>(fv);
                poll_flags(fv, oflag, lane < 32, tag);
                raw_bar();
                oload(ic<0>{}); oload(ic<1>{}); oload(ic<2>{});
            }
            if constexpr (i + 1 < P2_NS) rd_stage(ic<(i + 1 < P2_NS ? i + 1 : 0)>{});
            if constexpr ((i & 3) == 0) { STAMP(10 + i / 4); }
            if constexpr (i < P2_ND) {
#pragma unroll
                for (int ct = 0; ct < 2; ++ct)
#pragma unroll
                    for (int tt = 0; tt < 2; ++tt) acc[ct][tt] = MFMA32(wfr[b][ct], hfr[b][tt], acc[ct][tt]);
                if constexpr (i == P2_ND - 1) {
                    reduce(gfin);
#pragma unroll
                    for (int a = 0; a < 2; ++a)
#pragma unroll
                        for (int c = 0; c < 2; ++c)
#pragma unroll
                            for (int r = 0; r < 16; ++r) acc[a][c][r] = 0.f;
                }
            } else {
                constexpr int st = i - P2_ND;
                pin2(of[st % 3][0], of[st % 3][1]);
#pragma unroll
                for (int ct = 0; ct < 2; ++ct)
#pragma unroll
                    for (int tt = 0; tt < 2; ++tt) acc[ct][tt] = MFMA32(wfr[b][ct], __builtin_bit_cast(bf16x8, of[st % 3][tt]), acc[ct][tt]);
                if constexpr (st + 3 < 8) oload(ic<(st + 3 < 8) ? st + 3 : 0>{});
            }
        });
        STAMP(7);
        const int tok = ti * 64 + (w >> 1) * 32 + t32, col0 = cj * 64 + (w & 1) * 32 + 8 * hh;
        const u32x4 xa = *(const GAS u32x4*)((const GAS char*)x + (size_t)tok * 896 + col0 * 2), xb = *(const GAS u32x4*)((const GAS char*)x + (size_t)tok * 896 + col0 * 2 + 32);
        lds_bar();
        f32x16 sfin;
        reduce(sfin);
        unsigned sp[8];
#pragma unroll
        for (int j = 0; j < 8; ++j) {
            float r2[2];
#pragma unroll
            for (int e = 0; e < 2; ++e) { const float f = rbf(gfin[2 * j + e]), gt = rbf(f * __builtin_amdgcn_rcpf(1.0f + __expf(-f))); r2[e] = rbf(sfin[2 * j + e]) * gt; }
            sp[j] = pk_bf16(r2[0], r2[1]);
        }
#pragma unroll
        for (int n = 0; n < 2; ++n) {
            const auto r0 = __builtin_amdgcn_permlane32_swap(sp[4 * n], sp[4 * n + 2], false, false), r1 = __builtin_amdgcn_permlane32_swap(sp[4 * n + 1], sp[4 * n + 3], false, false);
            const u32x4 sg = {r0[0], r1[0], r0[1], r1[1]}, xx = n ? xb : xa;
            u32x4 o;
#pragma unroll
            for (int j = 0; j < 4; ++j) o[j] = pk_bf16(lo_f(xx[j]) + lo_f(sg[j]), hi_f(xx[j]) + hi_f(sg[j]));
            *(GAS u32x4*)((GAS char*)out + (size_t)tok * 896 + col0 * 2 + n * 32) = o;
        }
        STAMP(8);
        if (w == 0) STAMP_END(1);
        return;
    }

    const int wm = wid >> 2, wn = wid & 3;
    const int t32 = lane & 31, hh = lane >> 5;
    u32x4 xv[7]; u32x2 ev; ld8_sc1(ev, ws + WS_E);
    {
        const char* xp = (const char*)x + (size_t)(rt * 64 + (tid >> 3)) * 896 + (tid & 7) * 16;
        asm volatile("global_load_dwordx4 %0, %7, off\n\tglobal_load_dwordx4 %1, %7, off offset:128\n\tglobal_load_dwordx4 %2, %7, off offset:256\n\tglobal_load_dwordx4 %3, %7, off offset:384\n\t"
                     "global_load_dwordx4 %4, %7, off offset:512\n\tglobal_load_dwordx4 %5, %7, off offset:640\n\tglobal_load_dwordx4 %6, %7, off offset:768"
                     : "=&v"(xv[0]), "=&v"(xv[1]), "=&v"(xv[2]), "=&v"(xv[3]), "=&v"(xv[4]), "=&v"(xv[5]), "=&v"(xv[6]) : "v"(xp) : "memory");
    }
    asm volatile("s_waitcnt vmcnt(0)" : "+v"(ev), "+v"(xv[0]), "+v"(xv[1]), "+v"(xv[2]), "+v"(xv[3]), "+v"(xv[4]), "+v"(xv[5]), "+v"(xv[6])::"memory");
    const unsigned tl_ = __builtin_amdgcn_readfirstlane(ev[0]) + 1u, th_ = __builtin_amdgcn_readfirstlane(ev[1]) + (tl_ == 0u ? 1u : 0u);
    const u32x2 tag = {tl_, th_};
    STAMP(1);
    {
        float v[56]; float s0 = 0.f, s1 = 0.f;
#pragma unroll
        for (int i = 0; i < 7; ++i)
#pragma unroll
            for (int j = 0; j < 4; ++j) { v[i * 8 + 2 * j] = lo_f(xv[i][j]); v[i * 8 + 2 * j + 1] = hi_f(xv[i][j]); s0 += v[i * 8 + 2 * j]; s1 += v[i * 8 + 2 * j + 1]; }
        const float mean = sum8(s0 + s1) * (1.0f / 448.f), mb = rbf(mean);
        float q0 = 0.f, q1 = 0.f;
#pragma unroll
        for (int i = 0; i < 28; ++i) { v[2 * i] -= mb; v[2 * i + 1] -= mb; q0 = __builtin_fmaf(v[2 * i], v[2 * i], q0); q1 = __builtin_fmaf(v[2 * i + 1], v[2 * i + 1], q1); }
        const float dm = mean - mb, var = __builtin_fmaf(-dm, dm, sum8(q0 + q1) * (1.0f / 448.f));
        const float den = rbf(__builtin_sqrtf(rbf(rbf(var) + 1e-5f))), rden = 1.0f / den;
        const int row = tid >> 3, c8 = tid & 7;
        LAS char* dst = lds + (((row >> 5) * 28 + (c8 >> 1)) * 64 + (c8 & 1) * 32 + ((row & 31) ^ c8)) * 16;
#pragma unroll
        for (int i = 0; i < 7; ++i) {
            u32x4 o;
#pragma unroll
            for (int j = 0; j < 4; ++j) { const unsigned dp = pk_bf16(v[i * 8 + 2 * j], v[i * 8 + 2 * j + 1]); o[j] = pk_bf16(lo_f(dp) * rden, hi_f(dp) * rden); }
            *(LAS u32x4*)(dst + i * 4096) = o;
        }
    }
    lds_bar();
    bf16x8 xf[28];
#pragma unroll
    for (int s = 0; s < 28; ++s) xf[s] = *(const LAS bf16x8*)(lds + ((wm * 28 + s) * 64 + hh * 32 + (t32 ^ ((2 * s + hh) & 7))) * 16);
    lds_bar();
    STAMP(2);
    {
        f32x16 accs[2];
        unsigned rdo, rds = 0;
        auto set_rd = [&](int u) {
            const int ln = opaque(lane), hh = ln >> 5, q4 = (ln & 15) >> 2, p4 = ln & 3, chh = (ln >> 4) & 1;
            const int kind = unit_kind(u);
            const int byte = (kind == 3) ? chh * 128 + wn * 32 + 8 * p4 : 56 * wn + 32 * chh + 8 * ((chh && p4 == 3) ? 2 : p4);
            const int rg = byte >> 7, sw = (kind == 3) ? (((q4 >> 1) & 1) | (rg << 1)) : (q4 & 2);
            rdo = (unsigned)(rg * 8192 + (8 * hh + q4) * 128 + (((((byte >> 5) & 3) ^ sw) << 5) | (byte & 31)));
        };
        bf16x8 wf[3];
        auto rd_frag = [&](auto FF) {
            constexpr int Fi = FF; constexpr int i = Fi / 4, kq = Fi % 4, slot = (i + 4) % 10;
            if constexpr (Fi % 28 == 0) set_rd(i / 7);
            if constexpr (kq == 0) rds = (unsigned)opaque((int)(rdo + slot * 16384));
            const LAS char* b = lds + rds + kq * 2048;
            wf[Fi % 3] = cat8(tr_read(b), tr_read(b + 512));
        };
        auto flag_unit = [&](int u) {
            const int kind = unit_kind(u), ui = unit_idx(u);
            const int lane = opaque(tid) & 63;
            char* f = (kind == 3) ? ws + WS_FH + ((size_t)((rt * 4 + wn) * 32 + ui) * 2 + wm) * 8 : ws + WS_FQ + ((size_t)(((4 * ui + wn) * 3 + kind) * 32 + rt) * 2 + wm) * 8;
            if (lane == 0) st8_wt(f, tag);
        };
        auto epilogue = [&](auto U) {
            constexpr int u = U;
            const f32x16& acc = accs[u & 1];
            const int kind = (u == 1) ? unit_kind(u) : (u == 0 ? (cg >> 2) : 3), ui = unit_idx(u);
            const int ln = opaque(tid) & 63, t32 = ln & 31, hh = ln >> 5;
            if (kind == 3) {
                unsigned hp[4];
#pragma unroll
                for (int j = 0; j < 4; ++j) {
                    const unsigned gp = pk_bf16(acc[2 * j], acc[2 * j + 1]), up = pk_bf16(acc[8 + 2 * j], acc[8 + 2 * j + 1]);
                    hp[j] = pk_bf16(lo_f(gp) * lo_f(up), hi_f(gp) * hi_f(up));
                }
                const auto r0 = __builtin_amdgcn_permlane32_swap(hp[0], hp[2], false, false), r1 = __builtin_amdgcn_permlane32_swap(hp[1], hp[3], false, false);
                st16_wt(ws + WS_H + ((size_t)((rt * 2 + wm) * 224 + ui * 8 + wn * 2 + hh) * 32 + t32) * 16, u32x4{r0[0], r1[0], r0[1], r1[1]});
            } else {
                unsigned p[8];
#pragma unroll
                for (int j = 0; j < 8; ++j) p[j] = pk_bf16(acc[2 * j], acc[2 * j + 1]);
                if (hh) { p[6] = (kind == 2) ? 0x00003f80u : 0u; p[7] = 0u; }
                char* base = ws + (kind == 0 ? WS_Q : kind == 1 ? WS_K : WS_V) + (size_t)((4 * ui + wn) * 128 + rt * 4 + wm * 2 + (t32 >> 4)) * 1024;
                const int tt = (kind == 2) ? ((t32 & 15) ^ (8 * hh)) : (t32 & 15);
#pragma unroll
                for (int n = 0; n < 2; ++n) {
                    const auto r0 = __builtin_amdgcn_permlane32_swap(p[4 * n], p[4 * n + 2], false, false), r1 = __builtin_amdgcn_permlane32_swap(p[4 * n + 1], p[4 * n + 3], false, false);
                    st16_wt(base + ((2 * n + hh) * 16 + tt) * 16, u32x4{r0[0], r1[0], r0[1], r1[1]});
                }
            }
        };
        const int head = xcd * 2 + (idx >> 4), qt = idx & 15, a = wid;
        u32x2 fq; u32x4 qv;
        const int tq = qt * 8 + a;
        auto qflag = [&]() -> const char* { int z = opaque(0); return ws + WS_FQ + ((size_t)((head * 3) * 32 + (tq >> 2)) * 2 + ((tq >> 1) & 1)) * 8 + z; };
        auto qsrc = [&]() -> const char* { return ws + WS_Q + (size_t)(head * 128 + tq) * 1024 + (opaque(tid) & 63) * 16; };
        raw_bar();
        rd_frag(ic<0>{}); rd_frag(ic<1>{});
        static_for<0, 4 * P1_NSUB>([&](auto FF) {
            constexpr int Fi = FF;
            if constexpr (Fi % 8 == 0 && Fi > 0) { raw_bar(); if constexpr (Fi / 8 < 18) { STAMP(10 + Fi / 8); } }
            if constexpr (Fi + 2 < 4 * P1_NSUB) rd_frag(ic<(Fi + 2 < 4 * P1_NSUB ? Fi + 2 : 0)>{});
            constexpr int u = Fi / 28;
            if constexpr (Fi % 28 == 0) {
#pragma unroll
                for (int r = 0; r < 16; ++r) accs[u & 1][r] = 0.f;
            }
            accs[u & 1] = MFMA32(wf[Fi % 3], xf[Fi % 28], accs[u & 1]);
            if constexpr (Fi % 28 == 2 && u >= 1) epilogue(ic<(u >= 1 ? u - 1 : 0)>{});
            if constexpr (Fi == 4 * P1_NSUB - 1) epilogue(ic<4>{});
            if constexpr (Fi % 28 == 22 && u >= 1) { wait_vm<0>(); flag_unit(u - 1); }
            if constexpr (Fi == 112 + 8) ld8_sc1(fq, qflag());
            if constexpr (Fi == 112 + 23) { wait_vm_pin<1>(fq); poll_flags(fq, qflag(), true, tag); ld16_sc1(qv, qsrc()); }
        });
        STAMP(3);
        asm volatile("s_waitcnt vmcnt(%1) lgkmcnt(0)\n\ts_barrier" : "+v"(qv) : "n"(1) : "memory");
        const int lane = opaque(tid) & 63, g = lane >> 4, q4 = (lane & 15) >> 2, p4 = lane & 3;
        constexpr unsigned LK = 0, LV = 24576;
        STAMP(4);
        const int t16 = lane & 15;
        const bf16x8 qf = __builtin_bit_cast(bf16x8, qv);
        f32x4 sc[17];
#pragma unroll
        for (int j = 0; j < 17; ++j) { const bf16x8 kf = *(const LAS bf16x8*)(lds + LK + (a + j) * 1024 + lane * 16); sc[j] = MFMA16(kf, qf, (f32x4{0.f, 0.f, 0.f, 0.f})); }
        SBAR(); asm volatile("s_nop 15"); SBAR();
        const float NEG = -1e30f, L2E = 1.4426950408889634f, SCALE = 0.18898223650461362f;
#pragma unroll
        for (int r = 0; r < 4; ++r) { if (4 * g + r <= t16) sc[0][r] = NEG; if (4 * g + r > t16) sc[16][r] = NEG; }
        if (qt < 2) {
            asm volatile("" ::: "memory");
            const int first = 16 - qt * 8 - a;
#pragma unroll
            for (int j = 0; j < 16; ++j) if (j < first) sc[j] = f32x4{NEG, NEG, NEG, NEG};
        }
        float mx = max3f(sc[0][0], sc[0][1], sc[0][2]);
        mx = max3f(mx, sc[0][3], sc[1][0]);
        mx = max3f(mx, sc[1][1], sc[1][2]);
#pragma unroll
        for (int j = 2; j < 16; ++j) { mx = max3f(mx, sc[j][0], sc[j][1]); mx = max3f(mx, sc[j][2], sc[j][3]); }
        mx = max3f(mx, sc[16][0], sc[16][1]); mx = max3f(mx, sc[16][2], sc[16][3]);
        mx = max2f_pad(mx, sc[1][3]);
        { const auto r = __builtin_amdgcn_permlane16_swap(__float_as_uint(mx), __float_as_uint(mx), false, false); mx = max2f_pad(__uint_as_float(r[0]), __uint_as_float(r[1])); }
        { const auto r = __builtin_amdgcn_permlane32_swap(__float_as_uint(mx), __float_as_uint(mx), false, false); mx = max2f_pad(__uint_as_float(r[0]), __uint_as_float(r[1])); }
        wait_vm<0>(); flag_unit(4);
        const float nm = -rbf(rbf(mx) * SCALE) * L2E;
        const f32x2 nm2 = {nm, nm}, l2 = {L2E, L2E}, sc2 = {SCALE, SCALE};
        u32x2 pp[18];
#pragma unroll
        for (int j = 0; j < 17; ++j) {
            unsigned pk[2];
#pragma unroll
            for (int r = 0; r < 2; ++r) {
                const unsigned p1 = pk_bf16(sc[j][2 * r], sc[j][2 * r + 1]);
                const f32x2 s1 = f32x2{lo_f(p1), hi_f(p1)} * sc2;
                const unsigned p2 = pk_bf16(s1[0], s1[1]);
                const f32x2 e = __builtin_elementwise_fma(f32x2{lo_f(p2), hi_f(p2)}, l2, nm2);
                pk[r] = pk_bf16(__builtin_amdgcn_exp2f(e[0]), __builtin_amdgcn_exp2f(e[1]));
            }
            pp[j] = u32x2{pk[0], pk[1]};
        }
        pp[17] = u32x2{0u, 0u};
        f32x4 o0 = {0.f, 0.f, 0.f, 0.f}, o1 = {0.f, 0.f, 0.f, 0.f};
        const int vkey = 4 * g + q4;
        const unsigned va0 = LV + a * 1024 + (((p4 >> 1) * 16 + (vkey ^ (8 * (p4 >> 1)))) * 16) + (p4 & 1) * 8;
#pragma unroll
        for (int jp = 0; jp < 9; ++jp) {
            const int ja = 2 * jp, jb = (jp < 8) ? 2 * jp + 1 : 2 * jp;
            const bf16x8 pf = __builtin_bit_cast(bf16x8, (u32x4{pp[2 * jp][0], pp[2 * jp][1], pp[2 * jp + 1][0], pp[2 * jp + 1][1]}));
            const bf16x8 v0 = cat8(tr_read(lds + va0 + ja * 1024), tr_read(lds + va0 + jb * 1024));
            const bf16x8 v1 = cat8(tr_read(lds + va0 + ja * 1024 + 512), tr_read(lds + va0 + jb * 1024 + 512));
            o0 = MFMA16(v0, pf, o0); o1 = MFMA16(v1, pf, o1);
        }
        const float l = __shfl(o1[0], 48 + t16), rl = 1.0f / l;
        u32x4 ov = {pk_bf16(o0[0] * rl, o0[1] * rl), pk_bf16(o0[2] * rl, o0[3] * rl), pk_bf16(o1[0] * rl, o1[1] * rl), pk_bf16(o1[2] * rl, o1[3] * rl)};
        if (g == 3) { ov[2] = 0u; ov[3] = 0u; }
        const int tl = qt * 8 + a;
        STAMP(5);
        st16_wt(ws + WS_O + ((size_t)((tl >> 1) * 16 + head) * 4 + g) * 512 + ((tl & 1) * 16 + t16) * 16, ov);
        wait_vm<0>();
        if (lane == 0) st8_wt(ws + WS_FO + ((size_t)(((tl >> 2) * 2 + (head & 1)) * 8 + (head >> 1)) * 4 + (tl & 3)) * 8, tag);
        STAMP(6);
        if (wid == 0) STAMP_END(0);
    }
}

extern "C" void kernel_launch(void* const* d_in, const int* in_sizes, int n_in, void* d_out, int out_size, void* d_ws, size_t ws_size, hipStream_t stream) {
    const u16* x = (const u16*)d_in[0]; const u16* wqkv = (const u16*)d_in[1]; const u16* wo = (const u16*)d_in[2];
    const u16* wg = (const u16*)d_in[4]; const u16* wu = (const u16*)d_in[5]; const u16* wd = (const u16*)d_in[6];
    fused_kernel<<<256, 768, 0, stream>>>(x, wqkv, wo, wg, wu, wd, (char*)d_ws, (u16*)d_out);
}
```

```cpp
#define WARM 1
#define NO_PRIO 1
#include <hip/hip_runtime.h>
#include <stdint.h>
#include <type_traits>

typedef unsigned short u16;
typedef unsigned long long u64;
typedef short bf16x8 __attribute__((ext_vector_type(8)));
typedef short s16x4 __attribute__((ext_vector_type(4)));
typedef float f32x4 __attribute__((ext_vector_type(4)));
typedef float f32x16 __attribute__((ext_vector_type(16)));
typedef float f32x2 __attribute__((ext_vector_type(2)));
typedef unsigned u32x4 __attribute__((ext_vector_type(4)));
typedef unsigned u32x2 __attribute__((ext_vector_type(2)));
typedef __bf16 bf2 __attribute__((ext_vector_type(2)));
#define LAS __attribute__((address_space(3)))
#define GAS __attribute__((address_space(1)))
#define DI __device__ __forceinline__

constexpr size_t WS_E = 0;
constexpr size_t WS_FS = 4096;
constexpr size_t WS_FQ = 8192;
constexpr size_t WS_FH = 32768;
constexpr size_t WS_FO = 98304;
constexpr size_t WS_Q = 131072;
constexpr size_t WS_K = WS_Q + 2097152;
constexpr size_t WS_V = WS_K + 2097152;
constexpr size_t WS_H = WS_V + 2097152;
constexpr size_t WS_O = WS_H + 7340032;
constexpr size_t WS_END = WS_O + 2097152;

template <int N> using ic = std::integral_constant<int, N>;
template <int B, int E, class Fn> DI void static_for(Fn&& f) { if constexpr (B < E) { f(ic<B>{}); static_for<B + 1, E>(f); } }

DI unsigned pk_bf16(float a, float b) { f32x2 v = {a, b}; bf2 r = __builtin_convertvector(v, bf2); return __builtin_bit_cast(unsigned, r); }
DI float lo_f(unsigned p) { return __uint_as_float(p << 16); }
DI float hi_f(unsigned p) { return __uint_as_float(p & 0xffff0000u); }
DI float rbf(float a) { return lo_f(pk_bf16(a, 0.f)); }
#define MFMA16(a, b, c) __builtin_amdgcn_mfma_f32_16x16x32_bf16((a), (b), (c), 0, 0, 0)
#define MFMA32(a, b, c) __builtin_amdgcn_mfma_f32_32x32x16_bf16((a), (b), (c), 0, 0, 0)
#define SBAR() __builtin_amdgcn_sched_barrier(0)
DI void glds16(const void* g, unsigned lds_base) {
    unsigned sv;
    asm volatile("s_mov_b32 %0, m0\n\ts_mov_b32 m0, %2\n\ts_nop 0\n\tglobal_load_lds_dwordx4 %1, off\n\ts_mov_b32 m0, %0" : "=&s"(sv) : "v"(g), "s"(lds_base) : "memory");
}
DI void glds16_sc1(const void* g, unsigned lds_base) {
    unsigned sv;
    asm volatile("s_mov_b32 %0, m0\n\ts_mov_b32 m0, %2\n\ts_nop 0\n\tglobal_load_lds_dwordx4 %1, off sc1\n\ts_mov_b32 m0, %0" : "=&s"(sv) : "v"(g), "s"(lds_base) : "memory");
}
typedef int i32x4 __attribute__((ext_vector_type(4)));
DI i32x4 make_srd(const void* p, unsigned bytes) { const uintptr_t a = (uintptr_t)p; return i32x4{(int)(unsigned)a, (int)((unsigned)(a >> 32) & 0xffffu), (int)bytes, 0x00020000}; }
DI void blds16(i32x4 srd, unsigned voff, unsigned soff, unsigned lds_base) {
    asm volatile("s_mov_b32 m0, %3\n\ts_nop 0\n\tbuffer_load_dwordx4 %0, %1, %2 offen lds" ::"v"(voff), "s"(srd), "s"(soff), "s"(lds_base) : "memory");
}
DI void blds16_sc1(i32x4 srd, unsigned voff, unsigned soff, unsigned lds_base) {
    asm volatile("s_mov_b32 m0, %3\n\ts_nop 0\n\tbuffer_load_dwordx4 %0, %1, %2 offen sc1 lds" ::"v"(voff), "s"(srd), "s"(soff), "s"(lds_base) : "memory");
}
template <int N> DI void wait_bar() { asm volatile("s_waitcnt vmcnt(%0) lgkmcnt(0)\n\ts_barrier" ::"n"(N) : "memory"); }
template <int N> DI void wait_vm() { asm volatile("s_waitcnt vmcnt(%0)" ::"n"(N) : "memory"); }
DI void lds_bar() { asm volatile("s_waitcnt lgkmcnt(0)\n\ts_barrier" ::: "memory"); }
DI void raw_bar() { asm volatile("s_barrier" ::: "memory"); }
typedef short v4i16_t __attribute__((ext_vector_type(4)));
DI s16x4 tr_read(const LAS char* p) { return __builtin_bit_cast(s16x4, __builtin_amdgcn_ds_read_tr16_b64_v4i16((LAS v4i16_t*)p)); }
DI bf16x8 cat8(s16x4 a, s16x4 b) { return __builtin_shufflevector(a, b, 0, 1, 2, 3, 4, 5, 6, 7); }
DI void st16_wt(void* p, u32x4 v) { asm volatile("global_store_dwordx4 %0, %1, off sc1\n\ts_nop 1" ::"v"(p), "v"(v) : "memory"); }
DI void st8_wt(void* p, u32x2 v) { asm volatile("global_store_dwordx2 %0, %1, off sc1" ::"v"(p), "v"(v) : "memory"); }
DI void ld2_sc1(u32x4& a, u32x4& b, const char* p0, const char* p1) {
    asm volatile("global_load_dwordx4 %0, %2, off sc1\n\tglobal_load_dwordx4 %1, %3, off sc1" : "=&v"(a), "=&v"(b) : "v"(p0), "v"(p1) : "memory"); }
DI void ld16_sc1(u32x4& a, const char* p) { asm volatile("global_load_dwordx4 %0, %1, off sc1" : "=&v"(a) : "v"(p) : "memory"); }
DI void ld2_plain(u32x4& a, u32x4& b, const char* p) { asm volatile("global_load_dwordx4 %0, %2, off\n\tglobal_load_dwordx4 %1, %2, off offset:32" : "=&v"(a), "=&v"(b) : "v"(p) : "memory"); }
DI void pin2(u32x4& a, u32x4& b) { asm volatile("" : "+v"(a), "+v"(b)::"memory"); }
DI void ld8_sc1(u32x2& v, const void* p) { asm volatile("global_load_dwordx2 %0, %1, off sc1" : "=&v"(v) : "v"(p) : "memory"); }
DI void ld8_sc1_wait(u32x2& v, const void* p) { asm volatile("global_load_dwordx2 %0, %1, off sc1\n\ts_waitcnt vmcnt(0)" : "=&v"(v) : "v"(p) : "memory"); }
DI void pin8(unsigned (&v)[14]) { asm volatile("" : "+v"(v[0]), "+v"(v[1]), "+v"(v[2]), "+v"(v[3]), "+v"(v[4]), "+v"(v[5]), "+v"(v[6]), "+v"(v[7])::"memory"); }
DI void pin1(u32x2& v) { asm volatile("" : "+v"(v)::"memory"); }
template <int N> DI void wait_vm_pin(u32x2& v) { asm volatile("s_waitcnt vmcnt(%1)" : "+v"(v) : "n"(N) : "memory"); }
DI int opaque(int v) { asm volatile("" : "+v"(v)); return v; }
DI float max3f(float a, float b, float c) { float r; asm("v_max3_f32 %0, %1, %2, %3" : "=v"(r) : "v"(a), "v"(b), "v"(c)); return r; }
DI float max2f_pad(float a, float b) { float r; asm("v_max_f32 %0, %1, %2\n\ts_nop 1" : "=v"(r) : "v"(a), "v"(b)); return r; }
template <int CTRL> DI float dpp_add(float v) { return v + __builtin_bit_cast(float, __builtin_amdgcn_update_dpp(0, __builtin_bit_cast(int, v), CTRL, 0xf, 0xf, true)); }
DI float sum8(float v) { v = dpp_add<0xB1>(v); v = dpp_add<0x4E>(v); return dpp_add<0x141>(v); }
DI unsigned poll_flags(u32x2& v, const void* p, bool use, u32x2 tag) {
    unsigned spins = 0;
    while (!__all(!use || (v[0] == tag[0] && v[1] == tag[1]))) {
        __builtin_amdgcn_s_sleep(2);
        ld8_sc1_wait(v, p);
        if (++spins > (1u << 18)) break;
    }
    return spins;
}

#define STAMP_DECL do { } while (0)
#define STAMP(n) do { } while (0)
#define STAMPV(n, val) do { (void)(val); } while (0)
#define STAMP_END(k) do { } while (0)

constexpr int P1_NSUB = 35, P1_NINT = 18;
constexpr int P2_ND = 28, P2_NS = 36, P2_SL = 7;
constexpr int p2_slot(int i) { constexpr int t[7] = {9, 3, 4, 5, 6, 7, 8}; return t[i % 7]; }
constexpr int p2_unit(int i) {
    constexpr int t[28] = {12, 16, 20, 24, 0, 3, 6, 9, 13, 17, 21, 25, 1, 4, 7, 10, 14, 18, 22, 26, 2, 5, 8, 11, 15, 19, 23, 27};
    return t[i];
}
constexpr int p2_ops(int s) { return s < P2_ND ? 4 : 2; }
constexpr int P2_POLLO = 19, P2_OLD = 24;
constexpr int p2_oissue(int st) { return st < 3 ? P2_OLD : P2_ND + st - 3; }
enum { G_S1 = 0  , G_S2 = 100  , G_S2W = 150  , G_KV = 200, G_FH = 300  , G_FQ = 310, G_FO = 311, G_O = 320  , G_X = 330,
       P_BAR = 1000  , P_CHK1 = 1100  , P_C = 1200, P_STEP = 1300  , P_CHK2 = 1400   };
struct LdSim { int pt, grp, cnt; bool seen, done;
    constexpr void op(int g, int n) { if (done) return; if (seen) cnt += n; if (g == grp) { seen = true; cnt = 0; } }
    constexpr void point(int p) { if (p == pt) done = true; } };
constexpr int ld_count(int pt, int grp) {
    LdSim m{pt, grp, 0, false, false};
    for (int i = 0; i < 6; ++i) m.op(G_S1 + i, 4);
    for (int n = 0; n < P1_NINT; ++n) {
        m.point(P_BAR + n);
        if (n == 14) m.point(P_CHK1 + 14);
        if (n == 0) for (int i = 6; i < 10; ++i) m.op(G_S1 + i, 4);
        if (n == 12) m.op(G_FQ, 1);
        if (n == 16) m.op(G_FH + 1, 1);
        if (n >= 1) { if (2 * n + 8 < P1_NSUB) m.op(G_S1 + 2 * n + 8, 4); if (2 * n + 9 < P1_NSUB) m.op(G_S1 + 2 * n + 9, 4); }
        if (n == 13) m.op(G_S2W + 0, 2);
        if (n == 14) m.op(G_KV + 0, 8);
        if (n == 15) { m.op(G_KV + 1, 4); m.op(G_S2W + 1, 2); }
        if (n == 16) { m.op(G_S2W + 2, 2); m.op(G_S2W + 3, 2); }
        if (n == 17) { m.op(G_S2W + 4, 2); m.op(G_S2W + 5, 2); }
    }
    m.point(P_C);
    m.op(G_S2W + 6, 2);
    m.point(P_CHK1 + 18);
    for (int i = 0; i < P2_SL; ++i) m.op(G_S2 + i, 2);
    m.op(G_FH + 2, 1);
    m.point(P_STEP - 1);
    for (int i = 0; i < P2_NS; ++i) {
        m.point(P_STEP + i);
        m.point(P_CHK2 + i);
        if (i + P2_SL < P2_NS) m.op(G_S2 + i + P2_SL, p2_ops(i + P2_SL));
        if (i == 7) m.op(G_FH + 3, 1);
        if (i == P2_POLLO) m.op(G_FO, 1);
        m.point(P_CHK2 + 100 + i);
        for (int st = 0; st < 8; ++st) if (p2_oissue(st) == i) m.op(G_O + st, 2);
        if (i == P2_ND + 5) m.op(G_X, 2);
    }
    return m.cnt;
}
constexpr int cmin(int a, int b) { return a < b ? a : b; }
constexpr int p1_wait(int n) { return ld_count(P_BAR + n, G_S1 + cmin(2 * n + 3, P1_NSUB - 1)); }
constexpr int p2_wait(int i) {
    int c = ld_count(P_STEP + i, G_S2 + i + 1);
    if (i + 1 >= P2_ND && i + 1 < P2_NS) c = cmin(c, ld_count(P_STEP + i, G_O + (i + 1 - P2_ND)));
    return i + 1 < P2_NS ? c : 0;
}

__global__ __launch_bounds__(768, 3) void fused_kernel(const u16* __restrict__ x, const u16* __restrict__ wqkv, const u16* __restrict__ wo, const u16* __restrict__ wg, const u16* __restrict__ wu,
                                                       const u16* __restrict__ wd, char* __restrict__ ws, u16* __restrict__ out) {
    __shared__ __attribute__((aligned(1024))) char lds_[163840];
    LAS char* lds = (LAS char*)lds_;
    const unsigned lds0 = (unsigned)(uintptr_t)lds;
    const int tid = threadIdx.x, lane = tid & 63, wid = __builtin_amdgcn_readfirstlane(tid >> 6);
    const int bid = blockIdx.x, xcd = bid & 7, idx = bid >> 3;
    const int cg = (xcd & 3) * 2 + (idx & 1), rt = (xcd >> 2) * 16 + (idx >> 1);
    const int g = lane >> 4;
    STAMP_DECL; STAMP(0);
    auto unit_kind = [&](int u) -> int { return u == 0 ? (cg < 4 ? 2 : 1) : (u == 1 && cg < 4) ? 0 : 3; };
    auto unit_idx = [&](int u) -> int { return u == 0 ? (cg & 3) : (cg < 4) ? (u == 1 ? cg : 3 * cg + u - 2) : 12 + 4 * (cg - 4) + u - 1; };

    if (wid >= 8) {
        const int w = wid - 8;
        const i32x4 srd_g = make_srd(wg, 448 * 3584), srd_u = make_srd(wu, 448 * 3584), srd_q = make_srd(wqkv, 448 * 2688), srd_ws = make_srd(ws, (unsigned)WS_END);
        const int lr = lane >> 3, ls = lane & 7;
        unsigned vg[2], vq[2];
#pragma unroll
        for (int rg = 0; rg < 2; ++rg) {
            vg[rg] = (unsigned)(lr * 3584 + (((ls >> 1) ^ (((lr >> 1) & 1) | (rg << 1))) * 32) + (ls & 1) * 16);
            const int ch = rg * 8 + ((ls >> 1) ^ (lr & 2)) * 2 + (ls & 1);
            vq[rg] = (unsigned)(lr * 2688 + (ch < 13 ? ch : 13) * 16);
        }
        auto issue1 = [&](auto I) {
            constexpr int i = I; constexpr int u = i / 7, ks = i % 7, slot = (i + 4) % 10;
            const int kind = unit_kind(u), ui = unit_idx(u);
            const unsigned dst = lds0 + slot * 16384 + w * 2048;
            if (kind == 3) {
                const unsigned so = (unsigned)((ks * 64 + 16 * w) * 3584 + ui * 128);
                blds16(srd_g, vg[0], so, dst); blds16(srd_g, vg[0], so + 8 * 3584, dst + 1024);
                blds16(srd_u, vg[1], so, dst + 8192); blds16(srd_u, vg[1], so + 8 * 3584, dst + 8192 + 1024);
            } else {
                const unsigned so = (unsigned)((ks * 64 + 16 * w) * 2688 + kind * 896 + ui * 224);
                blds16(srd_q, vq[0], so, dst); blds16(srd_q, vq[0], so + 8 * 2688, dst + 1024);
                blds16(srd_q, vq[1], so, dst + 8192); blds16(srd_q, vq[1], so + 8 * 2688, dst + 8192 + 1024);
            }
        };
        u32x2 ev; ld8_sc1(ev, ws + WS_E);
        static_for<0, 6>(issue1);
        unsigned wm_[14];
#pragma unroll
        for (int u = 0; u < 5; ++u) {
            const int kind = unit_kind(u), ui = unit_idx(u);
            const int r = (rt & 15) * 28 + w * 7 + (lane >> 3 < 7 ? lane >> 3 : 6), c = lane & 3;
            const char* p = (kind == 3) ? (const char*)((c & 1) ? wu : wg) + (size_t)r * 3584 + ui * 128 : (const char*)wqkv + (size_t)r * 2688 + kind * 896 + ui * 224 + (c == 0 ? 0 : c == 1 ? 92 : c == 2 ? 128 : 220);
            asm volatile("global_load_dword %0, %1, off" : "=&v"(wm_[u]) : "v"(p) : "memory");
        }
#pragma unroll
        for (int k = 0; k < 2; ++k) {
            const int l = (idx * 4 + w) * 98 + k * 64 + lane; const int lc = (k * 64 + lane < 98) ? l : (idx * 4 + w) * 98;
            asm volatile("global_load_dword %0, %1, off" : "=&v"(wm_[5 + k]) : "v"((const char*)wd + (size_t)lc * 128) : "memory");
        }
        {
            const int l = (idx * 4 + w) * 25 + (lane < 25 ? lane : 0); const int lc = l < 3136 ? l : 0;
            asm volatile("global_load_dword %0, %1, off" : "=&v"(wm_[7]) : "v"((const char*)wo + (size_t)lc * 128) : "memory");
        }
        wait_vm_pin<32>(ev);
        const unsigned tl = __builtin_amdgcn_readfirstlane(ev[0]) + 1u, th = __builtin_amdgcn_readfirstlane(ev[1]) + (tl == 0u ? 1u : 0u);
        const u32x2 tag = {tl, th};
        if (w == 0 && lane == 0) st8_wt(ws + WS_FS + bid * 8, tag);
        const int head = xcd * 2 + (idx >> 4), qt = idx & 15;
        const bool gemm = idx < 28;
        const int ti = gemm ? xcd * 4 + idx / 7 : 0, cj = gemm ? idx % 7 : 0;
        const int wr = lane >> 3, wsl = lane & 7;
        const int wcb = cj * 128 + ((((wsl >> 1) ^ (wr & 2)) << 1) | (wsl & 1)) * 16;
        const i32x4 srd_d = make_srd(wd, 1792 * 896), srd_o = make_srd(wo, 448 * 896);
        const unsigned vd = (unsigned)(wr * 896 + wcb), vl = (unsigned)(lane * 16);
        unsigned vo[2];
#pragma unroll
        for (int i = 0; i < 2; ++i) { const int d = (wr < 4 ? 0 : 12) + 4 * (2 * (w & 1) + i) + wr; vo[i] = (unsigned)(((w >> 1) * 28 + (d < 28 ? d : 27)) * 896 + wcb); }
        const unsigned hso = (unsigned)(WS_H + ((size_t)(ti * 2) * 224 + 2 * w) * 512);
        const unsigned ring = lds0 + w * 4096;
        auto issue2h = [&](auto I) {
            constexpr int i = I; constexpr int so = p2_slot(i) * 16384, u = p2_unit(i);
            blds16_sc1(srd_ws, vl, hso + u * 4096, ring + so);
            blds16_sc1(srd_ws, vl, hso + u * 4096 + 224 * 512, ring + so + 1024);
        };
        auto issue2w = [&](auto I) {
            constexpr int i = I; constexpr int so = p2_slot(i) * 16384;
            if constexpr (i < P2_ND) {
                constexpr int u = p2_unit(i);
                blds16(srd_d, vd, (unsigned)((u * 64 + 16 * w) * 896), ring + so + 2048);
                blds16(srd_d, vd, (unsigned)((u * 64 + 16 * w + 8) * 896), ring + so + 3072);
            } else {
                blds16(srd_o, vo[0], (unsigned)((i - P2_ND) * 56 * 896), ring + so + 2048);
                blds16(srd_o, vo[1], (unsigned)((i - P2_ND) * 56 * 896), ring + so + 3072);
            }
        };
        auto issue2 = [&](auto I) { constexpr int i = I; if constexpr (i < P2_ND) issue2h(I); issue2w(I); };
        auto issue_kv = [&](int j0, int j1) {
#pragma unroll
            for (int j = j0; j < j1; ++j) {
                const int k = 4 * j + w, lt = k < 24 ? k : k - 24; int at = qt * 8 - 16 + lt; at = at < 0 ? 0 : at;
                blds16_sc1(srd_ws, vl, (unsigned)((k < 24 ? WS_K : WS_V) + (size_t)(head * 128 + at) * 1024), lds0 + k * 1024);
            }
        };
        int fu = 0;
        static_for<0, 28>([&](auto I) { if ((lane >> 1) == (int)I) fu = p2_unit(I); });
        const char* hflag = ws + WS_FH + ((size_t)((ti * 4 + w) * 32 + fu) * 2 + (lane & 1)) * 8;
        const char* oflag = ws + WS_FO + ((size_t)(ti * 2 + (w >> 1)) * 32 + (lane & 31)) * 8;
        const char* kflag;
        { const int li = lane < 12 ? lane : lane < 24 ? lane - 12 : 0; int r = 2 * qt - 4 + (li >> 1); r = r < 0 ? 0 : r;
          kflag = ws + WS_FQ + ((size_t)((head * 3 + (lane < 12 ? 1 : 2)) * 32 + r) * 2 + (li & 1)) * 8; }
        u32x2 fv, fq;
        raw_bar();
        raw_bar();
        static_for<0, P1_NINT>([&](auto N) {
            constexpr int n = N;
            wait_vm<p1_wait(n)>();
            if constexpr (n == 2) pin8(wm_);
            STAMP(20 + n);
            raw_bar();
            if constexpr (n == 14) { wait_vm_pin<ld_count(P_CHK1 + 14, G_FQ)>(fq); const unsigned sp_ = poll_flags(fq, kflag, lane < 24, tag); STAMPV(50, sp_); }
            if constexpr (n == 0) static_for<6, 10>(issue1);
            if constexpr (n == 12) ld8_sc1(fq, kflag);
            if constexpr (n == 16) ld8_sc1(fv, hflag);
            if constexpr (n >= 1 && 2 * n + 8 < P1_NSUB) issue1(ic<(n >= 1 && 2 * n + 8 < P1_NSUB) ? 2 * n + 8 : 0>{});
            if constexpr (n >= 1 && 2 * n + 9 < P1_NSUB) issue1(ic<(n >= 1 && 2 * n + 9 < P1_NSUB) ? 2 * n + 9 : 0>{});
            if constexpr (n == 13) issue2w(ic<0>{});
            if constexpr (n == 14) issue_kv(0, 8);
            if constexpr (n == 15) { issue_kv(8, 12); issue2w(ic<1>{}); }
            if constexpr (n == 16) { issue2w(ic<2>{}); issue2w(ic<3>{}); }
            if constexpr (n == 17) { issue2w(ic<4>{}); issue2w(ic<5>{}); }
        });
        wait_vm<ld_count(P_C, G_KV + 1)>();
        raw_bar();
        STAMP(1);
        if (!gemm) {
            if (bid == 255 && w == 0) {
                unsigned spins = 0;
                for (;;) {
                    bool ok = true;
#pragma unroll
                    for (int k = 0; k < 4; ++k) { u32x2 v; ld8_sc1_wait(v, ws + WS_FS + (k * 64 + lane) * 8); ok = ok && v[0] == tag[0] && v[1] == tag[1]; }
                    if (__all(ok) || ++spins > (1u << 18)) break;
                    __builtin_amdgcn_s_sleep(8);
                }
                if (lane == 0) st8_wt(ws + WS_E, tag);
            }
            return;
        }
        const int t32 = lane & 31, hh = lane >> 5, q4 = (lane & 15) >> 2, p4 = lane & 3;
        issue2w(ic<6>{});
        wait_vm_pin<ld_count(P_CHK1 + 18, G_FH + 1)>(fv); { const unsigned sp_ = poll_flags(fv, hflag, lane < 24, tag); STAMPV(51, sp_); }
        static_for<0, P2_SL>(issue2h);
        ld8_sc1(fv, hflag);
        STAMP(2);
        f32x16 acc[2][2];
#pragma unroll
        for (int a = 0; a < 2; ++a)
#pragma unroll
            for (int b = 0; b < 2; ++b)
#pragma unroll
                for (int r = 0; r < 16; ++r) acc[a][b][r] = 0.f;
        unsigned rbase[3][4];
#pragma unroll
        for (int z = 0; z < 3; ++z) {
#pragma unroll
            for (int ct = 0; ct < 2; ++ct) rbase[z][ct] = (unsigned)opaque(z * 65536 + w * 4096 + 2048 + (8 * hh + q4) * 128 + (((2 * (ct ^ (w & 1)) + (g & 1)) ^ (q4 & 2)) << 5) + p4 * 8);
            rbase[z][2] = (unsigned)opaque(z * 65536 + w * 4096 + (w >> 1) * 1024 + lane * 16); rbase[z][3] = (unsigned)opaque(z * 65536 + w * 4096 + (1 - (w >> 1)) * 1024 + lane * 16);
        }
        bf16x8 wfr[2][2], hfr[2][2];
        u32x4 of[3][2];
        const char* op = ws + WS_O + ((size_t)(ti * 2 * 16 + (w >> 1)) * 4 + 2 * (w & 1)) * 512 + lane * 16;
        auto rd_stage = [&](auto I) {
            constexpr int i = I; constexpr int so = p2_slot(i) * 16384, b = i & 1, z = so >> 16, im = so & 65535;
#pragma unroll
            for (int ct = 0; ct < 2; ++ct) wfr[b][ct] = cat8(tr_read(lds + rbase[z][ct] + im), tr_read(lds + rbase[z][ct] + im + 512));
            if constexpr (i < P2_ND) { hfr[b][0] = *(const LAS bf16x8*)(lds + rbase[z][2] + im); hfr[b][1] = *(const LAS bf16x8*)(lds + rbase[z][3] + im); }
        };
        auto oload = [&](auto ST) { constexpr int st = ST; ld2_sc1(of[st % 3][0], of[st % 3][1], op + st * 4096 + (w >> 1) * 32768, op + st * 4096 + (1 - (w >> 1)) * 32768); };
        f32x16 gfin;
        auto hand_over = [&](int base) {
            LAS f32x4* scr = (LAS f32x4*)(lds + base);
#pragma unroll
            for (int d = 1; d < 4; ++d) {
                const f32x16& a = acc[d & 1][d >> 1];
#pragma unroll
                for (int k = 0; k < 4; ++k) scr[(d - 1) * 1024 + w * 256 + k * 64 + lane] = f32x4{a[4 * k], a[4 * k + 1], a[4 * k + 2], a[4 * k + 3]};
            }
        };
        auto collect = [&](int base, f32x16& res) {
            const LAS f32x4* scr = (const LAS f32x4*)(lds + base);
#pragma unroll
            for (int d = 1; d < 4; ++d)
#pragma unroll
                for (int k = 0; k < 4; ++k) { const f32x4 v = scr[(d - 1) * 1024 + (w ^ d) * 256 + k * 64 + lane]; res[4 * k] += v[0]; res[4 * k + 1] += v[1]; res[4 * k + 2] += v[2]; res[4 * k + 3] += v[3]; }
        };
        const int tok = ti * 64 + (w >> 1) * 32 + t32, col0 = cj * 64 + (w & 1) * 32 + 8 * hh;
        u32x4 xa, xb;
        wait_vm<ld_count(P_STEP - 1, G_S2 + 0)>();
        rd_stage(ic<0>{});
        static_for<0, P2_NS>([&](auto I) {
            constexpr int i = I; constexpr int b = i & 1;
            asm volatile("s_waitcnt vmcnt(%0) lgkmcnt(0)" ::"n"(p2_wait(i)) : "memory");
            if constexpr (i == 5) { wait_vm_pin<ld_count(P_CHK2 + 5, G_FH + 2)>(fv); const unsigned sp_ = poll_flags(fv, hflag, lane < 40, tag); STAMPV(52, sp_); }
            if constexpr (i == 13) { wait_vm_pin<ld_count(P_CHK2 + 13, G_FH + 3)>(fv); const unsigned sp_ = poll_flags(fv, hflag, lane < 56, tag); STAMPV(53, sp_); }
            if constexpr (i + P2_SL < P2_NS) issue2(ic<(i + P2_SL < P2_NS ? i + P2_SL : 0)>{});
            if constexpr (i == 7) ld8_sc1(fv, hflag);
            if constexpr (i == P2_POLLO) ld8_sc1(fv, oflag);
            if constexpr (i == P2_OLD) {
                wait_vm_pin<ld_count(P_CHK2 + 100 + P2_OLD, G_FO)>(fv);
                { const unsigned sp_ = poll_flags(fv, oflag, lane < 32, tag); STAMPV(54, sp_); }
                raw_bar();
                oload(ic<0>{}); oload(ic<1>{}); oload(ic<2>{});
            }
            if constexpr (i + 1 < P2_NS) rd_stage(ic<(i + 1 < P2_NS ? i + 1 : 0)>{});
            if constexpr ((i & 3) == 0) { STAMP(10 + i / 4); }
            if constexpr (i < P2_ND) {
#pragma unroll
                for (int ct = 0; ct < 2; ++ct)
#pragma unroll
                    for (int tt = 0; tt < 2; ++tt) acc[ct][tt] = MFMA32(wfr[b][ct], hfr[b][tt], acc[ct][tt]);
                if constexpr (i == P2_ND - 1) {
                    hand_over(0); gfin = acc[0][0];
#pragma unroll
                    for (int a = 0; a < 2; ++a)
#pragma unroll
                        for (int c = 0; c < 2; ++c)
#pragma unroll
                            for (int r = 0; r < 16; ++r) acc[a][c][r] = 0.f;
                }
            } else {
                constexpr int st = i - P2_ND;
                pin2(of[st % 3][0], of[st % 3][1]);
#pragma unroll
                for (int ct = 0; ct < 2; ++ct)
#pragma unroll
                    for (int tt = 0; tt < 2; ++tt) acc[ct][tt] = MFMA32(wfr[b][ct], __builtin_bit_cast(bf16x8, of[st % 3][tt]), acc[ct][tt]);
                if constexpr (st + 3 < 8) oload(ic<(st + 3 < 8) ? st + 3 : 0>{});
                if constexpr (st == 5) ld2_plain(xa, xb, (const char*)x + (size_t)tok * 896 + col0 * 2);
            }
        });
        STAMP(7);
        hand_over(3 * 16384);
        f32x16 sfin = acc[0][0];
        asm volatile("s_waitcnt vmcnt(0) lgkmcnt(0)\n\ts_barrier" : "+v"(xa), "+v"(xb)::"memory");
        collect(0, gfin); collect(3 * 16384, sfin);
        unsigned sp[8];
#pragma unroll
        for (int j = 0; j < 8; ++j) {
            float r2[2];
#pragma unroll
            for (int e = 0; e < 2; ++e) { const float f = rbf(gfin[2 * j + e]), gt = rbf(f * __builtin_amdgcn_rcpf(1.0f + __expf(-f))); r2[e] = rbf(sfin[2 * j + e]) * gt; }
            sp[j] = pk_bf16(r2[0], r2[1]);
        }
#pragma unroll
        for (int n = 0; n < 2; ++n) {
            const auto r0 = __builtin_amdgcn_permlane32_swap(sp[4 * n], sp[4 * n + 2], false, false), r1 = __builtin_amdgcn_permlane32_swap(sp[4 * n + 1], sp[4 * n + 3], false, false);
            const u32x4 sg = {r0[0], r1[0], r0[1], r1[1]}, xx = n ? xb : xa;
            u32x4 o;
#pragma unroll
            for (int j = 0; j < 4; ++j) o[j] = pk_bf16(lo_f(xx[j]) + lo_f(sg[j]), hi_f(xx[j]) + hi_f(sg[j]));
            *(GAS u32x4*)((GAS char*)out + (size_t)tok * 896 + col0 * 2 + n * 32) = o;
        }
        if (bid == 0 && w == 0) {
            const unsigned long long until = __builtin_amdgcn_s_memrealtime() + 300ull;
            while (__builtin_amdgcn_s_memrealtime() < until) __builtin_amdgcn_s_sleep(4);
        }
        STAMP(8);
        if (w == 0) STAMP_END(1);
        return;
    }

    const int wm = wid >> 2, wn = wid & 3;
    const int t32 = lane & 31, hh = lane >> 5;
    u32x4 xv[7]; u32x2 ev; ld8_sc1(ev, ws + WS_E);
    {
        const char* xp = (const char*)x + (size_t)(rt * 64 + (tid >> 3)) * 896 + (tid & 7) * 16;
        asm volatile("global_load_dwordx4 %0, %7, off\n\tglobal_load_dwordx4 %1, %7, off offset:128\n\tglobal_load_dwordx4 %2, %7, off offset:256\n\tglobal_load_dwordx4 %3, %7, off offset:384\n\t"
                     "global_load_dwordx4 %4, %7, off offset:512\n\tglobal_load_dwordx4 %5, %7, off offset:640\n\tglobal_load_dwordx4 %6, %7, off offset:768"
                     : "=&v"(xv[0]), "=&v"(xv[1]), "=&v"(xv[2]), "=&v"(xv[3]), "=&v"(xv[4]), "=&v"(xv[5]), "=&v"(xv[6]) : "v"(xp) : "memory");
    }
    asm volatile("s_waitcnt vmcnt(0)" : "+v"(ev), "+v"(xv[0]), "+v"(xv[1]), "+v"(xv[2]), "+v"(xv[3]), "+v"(xv[4]), "+v"(xv[5]), "+v"(xv[6])::"memory");
    const unsigned tl_ = __builtin_amdgcn_readfirstlane(ev[0]) + 1u, th_ = __builtin_amdgcn_readfirstlane(ev[1]) + (tl_ == 0u ? 1u : 0u);
    const u32x2 tag = {tl_, th_};
    STAMP(1);
    {
        float v[56]; float s0 = 0.f, s1 = 0.f;
#pragma unroll
        for (int i = 0; i < 7; ++i)
#pragma unroll
            for (int j = 0; j < 4; ++j) { v[i * 8 + 2 * j] = lo_f(xv[i][j]); v[i * 8 + 2 * j + 1] = hi_f(xv[i][j]); s0 += v[i * 8 + 2 * j]; s1 += v[i * 8 + 2 * j + 1]; }
        const float mean = sum8(s0 + s1) * (1.0f / 448.f), mb = rbf(mean);
        float q0 = 0.f, q1 = 0.f;
#pragma unroll
        for (int i = 0; i < 28; ++i) { v[2 * i] -= mb; v[2 * i + 1] -= mb; q0 = __builtin_fmaf(v[2 * i], v[2 * i], q0); q1 = __builtin_fmaf(v[2 * i + 1], v[2 * i + 1], q1); }
        const float dm = mean - mb, var = __builtin_fmaf(-dm, dm, sum8(q0 + q1) * (1.0f / 448.f));
        const float den = rbf(__builtin_sqrtf(rbf(rbf(var) + 1e-5f))), rden = 1.0f / den;
        const int row = tid >> 3, c8 = tid & 7;
        LAS char* dst = lds + (((row >> 5) * 28 + (c8 >> 1)) * 64 + (c8 & 1) * 32 + ((row & 31) ^ c8)) * 16;
#pragma unroll
        for (int i = 0; i < 7; ++i) {
            u32x4 o;
#pragma unroll
            for (int j = 0; j < 4; ++j) { const unsigned dp = pk_bf16(v[i * 8 + 2 * j], v[i * 8 + 2 * j + 1]); o[j] = pk_bf16(lo_f(dp) * rden, hi_f(dp) * rden); }
            *(LAS u32x4*)(dst + i * 4096) = o;
        }
    }
    lds_bar();
    bf16x8 xf[28];
#pragma unroll
    for (int s = 0; s < 28; ++s) xf[s] = *(const LAS bf16x8*)(lds + ((wm * 28 + s) * 64 + hh * 32 + (t32 ^ ((2 * s + hh) & 7))) * 16);
    lds_bar();
    STAMP(2);
    {
        f32x16 accs[2];
        unsigned rdo, rds = 0;
        auto set_rd = [&](int u) {
            const int ln = opaque(lane), hh = ln >> 5, q4 = (ln & 15) >> 2, p4 = ln & 3, chh = (ln >> 4) & 1;
            const int kind = unit_kind(u);
            const int byte = (kind == 3) ? chh * 128 + wn * 32 + 8 * p4 : 56 * wn + 32 * chh + 8 * ((chh && p4 == 3) ? 2 : p4);
            const int rg = byte >> 7, sw = (kind == 3) ? (((q4 >> 1) & 1) | (rg << 1)) : (q4 & 2);
            rdo = (unsigned)(rg * 8192 + (8 * hh + q4) * 128 + (((((byte >> 5) & 3) ^ sw) << 5) | (byte & 31)));
        };
        bf16x8 wf[3];
        auto rd_frag = [&](auto FF) {
            constexpr int Fi = FF; constexpr int i = Fi / 4, kq = Fi % 4, slot = (i + 4) % 10;
            if constexpr (Fi % 28 == 0) set_rd(i / 7);
            if constexpr (kq == 0) rds = (unsigned)opaque((int)(rdo + slot * 16384));
            const LAS char* b = lds + rds + kq * 2048;
            wf[Fi % 3] = cat8(tr_read(b), tr_read(b + 512));
        };
        auto flag_unit = [&](int u) {
            const int kind = unit_kind(u), ui = unit_idx(u);
            const int lane = opaque(tid) & 63;
            char* f = (kind == 3) ? ws + WS_FH + ((size_t)((rt * 4 + wn) * 32 + ui) * 2 + wm) * 8 : ws + WS_FQ + ((size_t)(((4 * ui + wn) * 3 + kind) * 32 + rt) * 2 + wm) * 8;
            if (lane == 0) st8_wt(f, tag);
        };
        auto epilogue = [&](auto U) {
            constexpr int u = U;
            const f32x16& acc = accs[u & 1];
            const int kind = (u <= 1) ? unit_kind(u) : 3, ui = unit_idx(u);
            const int ln = opaque(tid) & 63, t32 = ln & 31, hh = ln >> 5;
            if (kind == 3) {
                unsigned hp[4];
#pragma unroll
                for (int j = 0; j < 4; ++j) {
                    const unsigned gp = pk_bf16(acc[2 * j], acc[2 * j + 1]), up = pk_bf16(acc[8 + 2 * j], acc[8 + 2 * j + 1]);
                    hp[j] = pk_bf16(lo_f(gp) * lo_f(up), hi_f(gp) * hi_f(up));
                }
                const auto r0 = __builtin_amdgcn_permlane32_swap(hp[0], hp[2], false, false), r1 = __builtin_amdgcn_permlane32_swap(hp[1], hp[3], false, false);
                st16_wt(ws + WS_H + ((size_t)((rt * 2 + wm) * 224 + ui * 8 + wn * 2 + hh) * 32 + t32) * 16, u32x4{r0[0], r1[0], r0[1], r1[1]});
            } else {
                unsigned p[8];
#pragma unroll
                for (int j = 0; j < 8; ++j) p[j] = pk_bf16(acc[2 * j], acc[2 * j + 1]);
                if (hh) { p[6] = (kind == 2) ? 0x00003f80u : 0u; p[7] = 0u; }
                char* base = ws + (kind == 0 ? WS_Q : kind == 1 ? WS_K : WS_V) + (size_t)((4 * ui + wn) * 128 + rt * 4 + wm * 2 + (t32 >> 4)) * 1024;
                const int tt = (kind == 2) ? ((t32 & 15) ^ (8 * hh)) : (t32 & 15);
#pragma unroll
                for (int n = 0; n < 2; ++n) {
                    const auto r0 = __builtin_amdgcn_permlane32_swap(p[4 * n], p[4 * n + 2], false, false), r1 = __builtin_amdgcn_permlane32_swap(p[4 * n + 1], p[4 * n + 3], false, false);
                    st16_wt(base + ((2 * n + hh) * 16 + tt) * 16, u32x4{r0[0], r1[0], r0[1], r1[1]});
                }
            }
        };
        const int head = xcd * 2 + (idx >> 4), qt = idx & 15, a = wid;
        u32x2 fq; u32x4 qv;
        const int tq = qt * 8 + a;
        auto qflag = [&]() -> const char* { int z = opaque(0); return ws + WS_FQ + ((size_t)((head * 3) * 32 + (tq >> 2)) * 2 + ((tq >> 1) & 1)) * 8 + z; };
        auto qsrc = [&]() -> const char* { return ws + WS_Q + (size_t)(head * 128 + tq) * 1024 + (opaque(tid) & 63) * 16; };
        raw_bar();
        rd_frag(ic<0>{}); rd_frag(ic<1>{});
        static_for<0, 4 * P1_NSUB>([&](auto FF) {
            constexpr int Fi = FF;
            if constexpr (Fi % 8 == 0 && Fi > 0) { raw_bar(); if constexpr (Fi / 8 < 18) { STAMP(10 + Fi / 8); } }
            if constexpr (Fi + 2 < 4 * P1_NSUB) rd_frag(ic<(Fi + 2 < 4 * P1_NSUB ? Fi + 2 : 0)>{});
            constexpr int u = Fi / 28;
            if constexpr (Fi % 28 == 0) {
#pragma unroll
                for (int r = 0; r < 16; ++r) accs[u & 1][r] = 0.f;
            }
            accs[u & 1] = MFMA32(wf[Fi % 3], xf[Fi % 28], accs[u & 1]);
            if constexpr (Fi % 28 == 2 && u >= 1) epilogue(ic<(u >= 1 ? u - 1 : 0)>{});
            if constexpr (Fi == 4 * P1_NSUB - 1) epilogue(ic<4>{});
            if constexpr (Fi % 28 == 14 && u >= 1) { wait_vm<0>(); flag_unit(u - 1); }
            if constexpr (Fi == 112 + 15) ld8_sc1(fq, qflag());
            if constexpr (Fi == 112 + 24) { wait_vm_pin<0>(fq); poll_flags(fq, qflag(), true, tag); ld16_sc1(qv, qsrc()); }
        });
        STAMP(3);
        asm volatile("s_waitcnt vmcnt(%1) lgkmcnt(0)\n\ts_barrier" : "+v"(qv) : "n"(1) : "memory");
        const int lane = opaque(tid) & 63, g = lane >> 4, q4 = (lane & 15) >> 2, p4 = lane & 3;
        constexpr unsigned LK = 0, LV = 24576;
        STAMP(4);
        const int t16 = lane & 15;
        const bf16x8 qf = __builtin_bit_cast(bf16x8, qv);
        f32x4 sc[17];
#pragma unroll
        for (int j = 0; j < 17; ++j) { const bf16x8 kf = *(const LAS bf16x8*)(lds + LK + (a + j) * 1024 + lane * 16); sc[j] = MFMA16(kf, qf, (f32x4{0.f, 0.f, 0.f, 0.f})); }
        SBAR(); asm volatile("s_nop 15"); SBAR();
        const float NEG = -1e30f, L2E = 1.4426950408889634f, SCALE = 0.18898223650461362f;
#pragma unroll
        for (int r = 0; r < 4; ++r) { if (4 * g + r <= t16) sc[0][r] = NEG; if (4 * g + r > t16) sc[16][r] = NEG; }
        if (qt < 2) {
            asm volatile("" ::: "memory");
            const int first = 16 - qt * 8 - a;
#pragma unroll
            for (int j = 0; j < 16; ++j) if (j < first) sc[j] = f32x4{NEG, NEG, NEG, NEG};
        }
        float mx = max3f(sc[0][0], sc[0][1], sc[0][2]);
        mx = max3f(mx, sc[0][3], sc[1][0]);
        mx = max3f(mx, sc[1][1], sc[1][2]);
#pragma unroll
        for (int j = 2; j < 16; ++j) { mx = max3f(mx, sc[j][0], sc[j][1]); mx = max3f(mx, sc[j][2], sc[j][3]); }
        mx = max3f(mx, sc[16][0], sc[16][1]); mx = max3f(mx, sc[16][2], sc[16][3]);
        mx = max2f_pad(mx, sc[1][3]);
        { const auto r = __builtin_amdgcn_permlane16_swap(__float_as_uint(mx), __float_as_uint(mx), false, false); mx = max2f_pad(__uint_as_float(r[0]), __uint_as_float(r[1])); }
        { const auto r = __builtin_amdgcn_permlane32_swap(__float_as_uint(mx), __float_as_uint(mx), false, false); mx = max2f_pad(__uint_as_float(r[0]), __uint_as_float(r[1])); }
        wait_vm<0>(); flag_unit(4);
        const float nm = -rbf(rbf(mx) * SCALE) * L2E;
        const f32x2 nm2 = {nm, nm}, l2 = {L2E, L2E}, sc2 = {SCALE, SCALE};
        u32x2 pp[18];
#pragma unroll
        for (int j = 0; j < 17; ++j) {
            unsigned pk[2];
#pragma unroll
            for (int r = 0; r < 2; ++r) {
                const unsigned p1 = pk_bf16(sc[j][2 * r], sc[j][2 * r + 1]);
                const f32x2 s1 = f32x2{lo_f(p1), hi_f(p1)} * sc2;
                const unsigned p2 = pk_bf16(s1[0], s1[1]);
                const f32x2 e = __builtin_elementwise_fma(f32x2{lo_f(p2), hi_f(p2)}, l2, nm2);
                pk[r] = pk_bf16(__builtin_amdgcn_exp2f(e[0]), __builtin_amdgcn_exp2f(e[1]));
            }
            pp[j] = u32x2{pk[0], pk[1]};
        }
        pp[17] = u32x2{0u, 0u};
        f32x4 o0 = {0.f, 0.f, 0.f, 0.f}, o1 = {0.f, 0.f, 0.f, 0.f};
        const int vkey = 4 * g + q4;
        const unsigned va0 = LV + a * 1024 + (((p4 >> 1) * 16 + (vkey ^ (8 * (p4 >> 1)))) * 16) + (p4 & 1) * 8;
#pragma unroll
        for (int jp = 0; jp < 9; ++jp) {
            const int ja = 2 * jp, jb = (jp < 8) ? 2 * jp + 1 : 2 * jp;
            const bf16x8 pf = __builtin_bit_cast(bf16x8, (u32x4{pp[2 * jp][0], pp[2 * jp][1], pp[2 * jp + 1][0], pp[2 * jp + 1][1]}));
            const bf16x8 v0 = cat8(tr_read(lds + va0 + ja * 1024), tr_read(lds + va0 + jb * 1024));
            const bf16x8 v1 = cat8(tr_read(lds + va0 + ja * 1024 + 512), tr_read(lds + va0 + jb * 1024 + 512));
            o0 = MFMA16(v0, pf, o0); o1 = MFMA16(v1, pf, o1);
        }
        const float l = __shfl(o1[0], 48 + t16), rl = 1.0f / l;
        u32x4 ov = {pk_bf16(o0[0] * rl, o0[1] * rl), pk_bf16(o0[2] * rl, o0[3] * rl), pk_bf16(o1[0] * rl, o1[1] * rl), pk_bf16(o1[2] * rl, o1[3] * rl)};
        if (g == 3) { ov[2] = 0u; ov[3] = 0u; }
        const int tl = qt * 8 + a;
        STAMP(5);
        st16_wt(ws + WS_O + ((size_t)((tl >> 1) * 16 + head) * 4 + g) * 512 + ((tl & 1) * 16 + t16) * 16, ov);
        wait_vm<0>();
        if (lane == 0) st8_wt(ws + WS_FO + ((size_t)(((tl >> 2) * 2 + (head & 1)) * 8 + (head >> 1)) * 4 + (tl & 3)) * 8, tag);
        STAMP(6);
        if (wid == 0) STAMP_END(0);
    }
}

extern "C" void kernel_launch(void* const* d_in, const int* in_sizes, int n_in, void* d_out, int out_size, void* d_ws, size_t ws_size, hipStream_t stream) {
    const u16* x = (const u16*)d_in[0]; const u16* wqkv = (const u16*)d_in[1]; const u16* wo = (const u16*)d_in[2];
    const u16* wg = (const u16*)d_in[4]; const u16* wu = (const u16*)d_in[5]; const u16* wd = (const u16*)d_in[6];
    fused_kernel<<<256, 768, 0, stream>>>(x, wqkv, wo, wg, wu, wd, (char*)d_ws, (u16*)d_out);
}
```

```cpp
#include <hip/hip_runtime.h>
#include <stdint.h>
#include <type_traits>

typedef unsigned short u16;
typedef unsigned long long u64;
typedef short bf16x8 __attribute__((ext_vector_type(8)));
typedef short s16x4 __attribute__((ext_vector_type(4)));
typedef float f32x4 __attribute__((ext_vector_type(4)));
typedef float f32x16 __attribute__((ext_vector_type(16)));
typedef float f32x2 __attribute__((ext_vector_type(2)));
typedef unsigned u32x4 __attribute__((ext_vector_type(4)));
typedef unsigned u32x2 __attribute__((ext_vector_type(2)));
typedef __bf16 bf2 __attribute__((ext_vector_type(2)));
#define LAS __attribute__((address_space(3)))
#define GAS __attribute__((address_space(1)))
#define DI __device__ __forceinline__

constexpr size_t WS_E = 0;
constexpr size_t WS_FS = 4096;
constexpr size_t WS_FQ = 8192;
constexpr size_t WS_FH = 32768;
constexpr size_t WS_FO = 98304;
constexpr size_t WS_Q = 131072;
constexpr size_t WS_K = WS_Q + 2097152;
constexpr size_t WS_V = WS_K + 2097152;
constexpr size_t WS_H = WS_V + 2097152;
constexpr size_t WS_O = WS_H + 7340032;
constexpr size_t WS_END = WS_O + 2097152;

template <int N> using ic = std::integral_constant<int, N>;
template <int B, int E, class Fn> DI void static_for(Fn&& f) { if constexpr (B < E) { f(ic<B>{}); static_for<B + 1, E>(f); } }

DI unsigned pk_bf16(float a, float b) { f32x2 v = {a, b}; bf2 r = __builtin_convertvector(v, bf2); return __builtin_bit_cast(unsigned, r); }
DI float lo_f(unsigned p) { return __uint_as_float(p << 16); }
DI float hi_f(unsigned p) { return __uint_as_float(p & 0xffff0000u); }
DI float rbf(float a) { return lo_f(pk_bf16(a, 0.f)); }
#define MFMA16(a, b, c) __builtin_amdgcn_mfma_f32_16x16x32_bf16((a), (b), (c), 0, 0, 0)
#define MFMA32(a, b, c) __builtin_amdgcn_mfma_f32_32x32x16_bf16((a), (b), (c), 0, 0, 0)
#define SBAR() __builtin_amdgcn_sched_barrier(0)
typedef int i32x4 __attribute__((ext_vector_type(4)));
DI i32x4 make_srd(const void* p, unsigned bytes) { const uintptr_t a = (uintptr_t)p; return i32x4{(int)(unsigned)a, (int)((unsigned)(a >> 32) & 0xffffu), (int)bytes, 0x00020000}; }
DI void blds16(i32x4 srd, unsigned voff, unsigned soff, unsigned lds_base) {
    asm volatile("s_mov_b32 m0, %3\n\ts_nop 0\n\tbuffer_load_dwordx4 %0, %1, %2 offen lds" ::"v"(voff), "s"(srd), "s"(soff), "s"(lds_base) : "memory");
}
DI void blds16_sc1(i32x4 srd, unsigned voff, unsigned soff, unsigned lds_base) {
    asm volatile("s_mov_b32 m0, %3\n\ts_nop 0\n\tbuffer_load_dwordx4 %0, %1, %2 offen sc1 lds" ::"v"(voff), "s"(srd), "s"(soff), "s"(lds_base) : "memory");
}
template <int N> DI void wait_vm() { asm volatile("s_waitcnt vmcnt(%0)" ::"n"(N) : "memory"); }
DI void lds_bar() { asm volatile("s_waitcnt lgkmcnt(0)\n\ts_barrier" ::: "memory"); }
DI void raw_bar() { asm volatile("s_barrier" ::: "memory"); }
typedef short v4i16_t __attribute__((ext_vector_type(4)));
DI s16x4 tr_read(const LAS char* p) { return __builtin_bit_cast(s16x4, __builtin_amdgcn_ds_read_tr16_b64_v4i16((LAS v4i16_t*)p)); }
DI bf16x8 cat8(s16x4 a, s16x4 b) { return __builtin_shufflevector(a, b, 0, 1, 2, 3, 4, 5, 6, 7); }
DI void st16_wt(void* p, u32x4 v) { asm volatile("global_store_dwordx4 %0, %1, off sc1\n\ts_nop 1" ::"v"(p), "v"(v) : "memory"); }
DI void st8_wt(void* p, u32x2 v) { asm volatile("global_store_dwordx2 %0, %1, off sc1" ::"v"(p), "v"(v) : "memory"); }
DI void ld16_sc1(u32x4& a, const char* p) { asm volatile("global_load_dwordx4 %0, %1, off sc1" : "=&v"(a) : "v"(p) : "memory"); }
DI void ld2_plain(u32x4& a, u32x4& b, const char* p) { asm volatile("global_load_dwordx4 %0, %2, off\n\tglobal_load_dwordx4 %1, %2, off offset:32" : "=&v"(a), "=&v"(b) : "v"(p) : "memory"); }
DI void ld8_sc1(u32x2& v, const void* p) { asm volatile("global_load_dwordx2 %0, %1, off sc1" : "=&v"(v) : "v"(p) : "memory"); }
DI void ld8_sc1_wait(u32x2& v, const void* p) { asm volatile("global_load_dwordx2 %0, %1, off sc1\n\ts_waitcnt vmcnt(0)" : "=&v"(v) : "v"(p) : "memory"); }
DI void pin8(unsigned (&v)[8]) { asm volatile("" : "+v"(v[0]), "+v"(v[1]), "+v"(v[2]), "+v"(v[3]), "+v"(v[4]), "+v"(v[5]), "+v"(v[6]), "+v"(v[7])::"memory"); }
DI void pin1(u32x2& v) { asm volatile("" : "+v"(v)::"memory"); }
template <int N> DI void wait_vm_pin(u32x2& v) { asm volatile("s_waitcnt vmcnt(%1)" : "+v"(v) : "n"(N) : "memory"); }
DI int opaque(int v) { asm volatile("" : "+v"(v)); return v; }
DI float max3f(float a, float b, float c) { float r; asm("v_max3_f32 %0, %1, %2, %3" : "=v"(r) : "v"(a), "v"(b), "v"(c)); return r; }
DI float max2f_pad(float a, float b) { float r; asm("v_max_f32 %0, %1, %2\n\ts_nop 1" : "=v"(r) : "v"(a), "v"(b)); return r; }
template <int CTRL> DI float dpp_add(float v) { return v + __builtin_bit_cast(float, __builtin_amdgcn_update_dpp(0, __builtin_bit_cast(int, v), CTRL, 0xf, 0xf, true)); }
DI float sum8(float v) { v = dpp_add<0xB1>(v); v = dpp_add<0x4E>(v); return dpp_add<0x141>(v); }
DI void poll_flags(u32x2& v, const void* p, bool use, u32x2 tag) {
    unsigned spins = 0;
    while (!__all(!use || (v[0] == tag[0] && v[1] == tag[1]))) {
        __builtin_amdgcn_s_sleep(2);
        ld8_sc1_wait(v, p);
        if (++spins > (1u << 18)) break;
    }
}


constexpr int P1_NSUB = 35, P1_NINT = 18;
constexpr int P2_ND = 28, P2_NS = 36, P2_SL = 7;
constexpr int p2_slot(int i) { constexpr int t[7] = {9, 3, 4, 5, 6, 7, 8}; return t[i % 7]; }
constexpr int p2_unit(int i) {
    constexpr int t[28] = {12, 16, 20, 24, 0, 3, 6, 9, 13, 17, 21, 25, 1, 4, 7, 10, 14, 18, 22, 26, 2, 5, 8, 11, 15, 19, 23, 27};
    return t[i];
}
constexpr int p2_ops(int s) { return (s < P2_ND || s >= P2_ND + 6) ? 4 : 2; }
constexpr int P2_POLLO = 20, P2_OLD = 26, P2_OE = 6;
enum { G_S1 = 0  , G_S2 = 100  , G_S2W = 150  , G_KV = 200, G_FH = 300  , G_FQ = 310, G_FO = 311, G_O = 320  , G_X = 330,
       P_BAR = 1000  , P_CHK1 = 1100  , P_C = 1200, P_STEP = 1300  , P_CHK2 = 1400   };
struct LdSim { int pt, grp, cnt; bool seen, done;
    constexpr void op(int g, int n) { if (done) return; if (seen) cnt += n; if (g == grp) { seen = true; cnt = 0; } }
    constexpr void point(int p) { if (p == pt) done = true; } };
constexpr int ld_count(int pt, int grp) {
    LdSim m{pt, grp, 0, false, false};
    for (int i = 0; i < 6; ++i) m.op(G_S1 + i, 4);
    for (int n = 0; n < P1_NINT; ++n) {
        m.point(P_BAR + n);
        if (n == 14) m.point(P_CHK1 + 14);
        if (n == 0) for (int i = 6; i < 10; ++i) m.op(G_S1 + i, 4);
        if (n == 12) m.op(G_FQ, 1);
        if (n == 16) m.op(G_FH + 1, 1);
        if (n >= 1) { if (2 * n + 8 < P1_NSUB) m.op(G_S1 + 2 * n + 8, 4); if (2 * n + 9 < P1_NSUB) m.op(G_S1 + 2 * n + 9, 4); }
        if (n == 13) m.op(G_S2W + 0, 2);
        if (n == 14) m.op(G_KV + 0, 8);
        if (n == 15) { m.op(G_KV + 1, 4); m.op(G_S2W + 1, 2); }
        if (n == 16) { m.op(G_S2W + 2, 2); m.op(G_S2W + 3, 2); }
        if (n == 17) { m.op(G_S2W + 4, 2); m.op(G_S2W + 5, 2); }
    }
    m.point(P_C);
    m.op(G_S2W + 6, 2);
    m.point(P_CHK1 + 18);
    for (int i = 0; i < P2_SL; ++i) m.op(G_S2 + i, 2);
    m.op(G_FH + 2, 1);
    m.point(P_STEP - 1);
    for (int i = 0; i < P2_NS; ++i) {
        m.point(P_STEP + i);
        m.point(P_CHK2 + i);
        if (i + P2_SL < P2_NS) m.op(G_S2 + i + P2_SL, p2_ops(i + P2_SL));
        if (i == 7) m.op(G_FH + 3, 1);
        if (i == P2_POLLO) m.op(G_FO, 1);
        m.point(P_CHK2 + 100 + i);
        if (i == P2_OLD) for (int st = 0; st < P2_OE; ++st) m.op(G_O + st, 2);
        if (i == P2_ND + 2) m.op(G_X, 2);
    }
    return m.cnt;
}
constexpr int cmin(int a, int b) { return a < b ? a : b; }
constexpr int p1_wait(int n) { return ld_count(P_BAR + n, G_S1 + cmin(2 * n + 3, P1_NSUB - 1)); }
constexpr int p2_wait(int i) {
    int c = ld_count(P_STEP + i, G_S2 + i + 1);
    if (i + 1 >= P2_ND && i + 1 < P2_ND + P2_OE) c = cmin(c, ld_count(P_STEP + i, G_O + (i + 1 - P2_ND)));
    return i + 1 < P2_NS ? c : 0;
}

__global__ __launch_bounds__(768, 3) void fused_kernel(const u16* __restrict__ x, const u16* __restrict__ wqkv, const u16* __restrict__ wo, const u16* __restrict__ wg, const u16* __restrict__ wu,
                                                       const u16* __restrict__ wd, char* __restrict__ ws, u16* __restrict__ out) {
    __shared__ __attribute__((aligned(1024))) char lds_[163840];
    LAS char* lds = (LAS char*)lds_;
    const unsigned lds0 = (unsigned)(uintptr_t)lds;
    const int tid = threadIdx.x, lane = tid & 63, wid = __builtin_amdgcn_readfirstlane(tid >> 6);
    const int bid = blockIdx.x, xcd = bid & 7, idx = bid >> 3;
    const int cg = (xcd & 3) * 2 + (idx & 1), rt = (xcd >> 2) * 16 + (idx >> 1);
    const int g = lane >> 4;
    auto unit_kind = [&](int u) -> int { return u == 0 ? (cg < 4 ? 2 : 1) : (u == 1 && cg < 4) ? 0 : 3; };
    auto unit_idx = [&](int u) -> int { return u == 0 ? (cg & 3) : (cg < 4) ? (u == 1 ? cg : 3 * cg + u - 2) : 12 + 4 * (cg - 4) + u - 1; };

    if (wid >= 8) {
        const int w = wid - 8;
        const i32x4 srd_g = make_srd(wg, 448 * 3584), srd_u = make_srd(wu, 448 * 3584), srd_q = make_srd(wqkv, 448 * 2688), srd_ws = make_srd(ws, (unsigned)WS_END);
        const int lr = lane >> 3, ls = lane & 7;
        unsigned vg[2], vq[2];
#pragma unroll
        for (int rg = 0; rg < 2; ++rg) vg[rg] = (unsigned)(lr * 3584 + (((ls >> 1) ^ (((lr >> 1) & 1) | (rg << 1))) * 32) + (ls & 1) * 16);
        {
            const int o = 224 * (cg & 3), r0 = o & 127, l0 = o >> 7, b16 = 16 * ((((ls >> 1) ^ (lr & 2)) << 1) | (ls & 1)), t = b16 - (128 - r0), need = o + 224 - (l0 + 2) * 128;
            vq[0] = (unsigned)(lr * 2688 + (t < 0 ? o + b16 : t < need ? (l0 + 2) * 128 + t : o));
            vq[1] = (unsigned)(lr * 2688 + (l0 + 1) * 128 + b16);
        }
        auto issue1 = [&](auto I) {
            constexpr int i = I; constexpr int u = i / 7, ks = i % 7, slot = (i + 4) % 10;
            const int kind = unit_kind(u), ui = unit_idx(u);
            const unsigned dst = lds0 + slot * 16384 + w * 2048;
            if (kind == 3) {
                const unsigned so = (unsigned)((ks * 64 + 16 * w) * 3584 + ui * 128);
                blds16(srd_g, vg[0], so, dst); blds16(srd_g, vg[0], so + 8 * 3584, dst + 1024);
                blds16(srd_u, vg[1], so, dst + 8192); blds16(srd_u, vg[1], so + 8 * 3584, dst + 8192 + 1024);
            } else {
                const unsigned so = (unsigned)((ks * 64 + 16 * w) * 2688 + kind * 896);
                blds16(srd_q, vq[0], so, dst); blds16(srd_q, vq[0], so + 8 * 2688, dst + 1024);
                blds16(srd_q, vq[1], so, dst + 8192); blds16(srd_q, vq[1], so + 8 * 2688, dst + 8192 + 1024);
            }
        };
        u32x2 ev; ld8_sc1(ev, ws + WS_E);
        unsigned wm_[8];
#pragma unroll
        for (int u = 0; u < 5; ++u) {
            const int kind = unit_kind(u), ui = unit_idx(u);
            const int r = (rt & 15) * 28 + w * 7 + (lane >> 3 < 7 ? lane >> 3 : 6), c = lane & 3;
            const char* p = (kind == 3) ? (const char*)((c & 1) ? wu : wg) + (size_t)r * 3584 + ui * 128 : (const char*)wqkv + (size_t)r * 2688 + kind * 896 + ui * 224 + (c == 0 ? 0 : c == 1 ? 92 : c == 2 ? 128 : 220);
            asm volatile("global_load_dword %0, %1, off" : "=&v"(wm_[u]) : "v"(p) : "memory");
        }
        static_for<0, 6>(issue1);
#pragma unroll
        for (int k = 0; k < 2; ++k) {
            const int l = (idx * 4 + w) * 98 + k * 64 + lane; const int lc = (k * 64 + lane < 98) ? l : (idx * 4 + w) * 98;
            asm volatile("global_load_dword %0, %1, off" : "=&v"(wm_[5 + k]) : "v"((const char*)wd + (size_t)lc * 128) : "memory");
        }
        {
            const int l = (idx * 4 + w) * 25 + (lane < 25 ? lane : 0); const int lc = l < 3136 ? l : 0;
            asm volatile("global_load_dword %0, %1, off" : "=&v"(wm_[7]) : "v"((const char*)wo + (size_t)lc * 128) : "memory");
        }
        wait_vm_pin<32>(ev);
        const unsigned tl = __builtin_amdgcn_readfirstlane(ev[0]) + 1u, th = __builtin_amdgcn_readfirstlane(ev[1]) + (tl == 0u ? 1u : 0u);
        const u32x2 tag = {tl, th};
        const int head = xcd * 2 + (idx >> 4), qt = idx & 15;
        const bool gemm = idx < 28;
        const int ti = gemm ? xcd * 4 + idx / 7 : 0, cj = gemm ? idx % 7 : 0;
        const int wr = lane >> 3, wsl = lane & 7;
        const int wcb = cj * 128 + ((((wsl >> 1) ^ (wr & 2)) << 1) | (wsl & 1)) * 16;
        const i32x4 srd_d = make_srd(wd, 1792 * 896), srd_o = make_srd(wo, 448 * 896);
        const unsigned vd = (unsigned)(wr * 896 + wcb), vl = (unsigned)(lane * 16);
        unsigned vo[2];
#pragma unroll
        for (int i = 0; i < 2; ++i) { const int d = (wr < 4 ? 0 : 12) + 4 * (2 * (w & 1) + i) + wr; vo[i] = (unsigned)(((w >> 1) * 28 + (d < 28 ? d : 27)) * 896 + wcb); }
        const unsigned hso = (unsigned)(WS_H + ((size_t)(ti * 2) * 224 + 2 * w) * 512);
        const unsigned ring = lds0 + w * 4096;
        auto issue2h = [&](auto I) {
            constexpr int i = I; constexpr int so = p2_slot(i) * 16384, u = p2_unit(i);
            blds16_sc1(srd_ws, vl, hso + u * 4096, ring + so);
            blds16_sc1(srd_ws, vl, hso + u * 4096 + 224 * 512, ring + so + 1024);
        };
        auto issue2w = [&](auto I) {
            constexpr int i = I; constexpr int so = p2_slot(i) * 16384;
            if constexpr (i < P2_ND) {
                constexpr int u = p2_unit(i);
                blds16(srd_d, vd, (unsigned)((u * 64 + 16 * w) * 896), ring + so + 2048);
                blds16(srd_d, vd, (unsigned)((u * 64 + 16 * w + 8) * 896), ring + so + 3072);
            } else {
                blds16(srd_o, vo[0], (unsigned)((i - P2_ND) * 56 * 896), ring + so + 2048);
                blds16(srd_o, vo[1], (unsigned)((i - P2_ND) * 56 * 896), ring + so + 3072);
            }
        };
        const unsigned oso = (unsigned)(WS_O + ((size_t)(ti * 2 * 16 + (w >> 1)) * 4 + 2 * (w & 1)) * 512);
        auto issue2o = [&](auto I) {
            constexpr int i = I; constexpr int so = p2_slot(i) * 16384, st = i - P2_ND;
            blds16_sc1(srd_ws, vl, oso + st * 4096, ring + so);
            blds16_sc1(srd_ws, vl, oso + st * 4096 + 32768, ring + so + 1024);
        };
        auto issue2 = [&](auto I) { constexpr int i = I; if constexpr (i < P2_ND) issue2h(I); if constexpr (i >= P2_ND + P2_OE) issue2o(I); issue2w(I); };
        auto issue_kv = [&](int j0, int j1) {
#pragma unroll
            for (int j = j0; j < j1; ++j) {
                const int k = 4 * j + w, lt = k < 24 ? k : k - 24; int at = qt * 8 - 16 + lt; at = at < 0 ? 0 : at;
                blds16_sc1(srd_ws, vl, (unsigned)((k < 24 ? WS_K : WS_V) + (size_t)(head * 128 + at) * 1024), lds0 + k * 1024);
            }
        };
        int fu = 0;
        static_for<0, 28>([&](auto I) { if ((lane >> 1) == (int)I) fu = p2_unit(I); });
        const char* hflag = ws + WS_FH + ((size_t)((ti * 4 + w) * 32 + fu) * 2 + (lane & 1)) * 8;
        const char* oflag = ws + WS_FO + ((size_t)(ti * 2 + (w >> 1)) * 32 + (lane & 31)) * 8;
        const char* kflag;
        { const int li = lane < 12 ? lane : lane < 24 ? lane - 12 : 0; int r = 2 * qt - 4 + (li >> 1); r = r < 0 ? 0 : r;
          kflag = ws + WS_FQ + ((size_t)((head * 3 + (lane < 12 ? 1 : 2)) * 32 + r) * 2 + (li & 1)) * 8; }
        u32x2 fv, fq;
        raw_bar();
        if (w == 0 && lane == 0) st8_wt(ws + WS_FS + bid * 8, tag);
        raw_bar();
        static_for<0, P1_NINT>([&](auto N) {
            constexpr int n = N;
            wait_vm<p1_wait(n)>();
            if constexpr (n == 2) pin8(wm_);
            raw_bar();
            if constexpr (n == 14) { wait_vm_pin<ld_count(P_CHK1 + 14, G_FQ)>(fq); poll_flags(fq, kflag, lane < 24, tag); }
            if constexpr (n == 0) static_for<6, 10>(issue1);
            if constexpr (n == 12) ld8_sc1(fq, kflag);
            if constexpr (n == 16) ld8_sc1(fv, hflag);
            if constexpr (n >= 1 && 2 * n + 8 < P1_NSUB) issue1(ic<(n >= 1 && 2 * n + 8 < P1_NSUB) ? 2 * n + 8 : 0>{});
            if constexpr (n >= 1 && 2 * n + 9 < P1_NSUB) issue1(ic<(n >= 1 && 2 * n + 9 < P1_NSUB) ? 2 * n + 9 : 0>{});
            if constexpr (n == 13) issue2w(ic<0>{});
            if constexpr (n == 14) issue_kv(0, 8);
            if constexpr (n == 15) { issue_kv(8, 12); issue2w(ic<1>{}); }
            if constexpr (n == 16) { issue2w(ic<2>{}); issue2w(ic<3>{}); }
            if constexpr (n == 17) { issue2w(ic<4>{}); issue2w(ic<5>{}); }
        });
        wait_vm<ld_count(P_C, G_KV + 1)>();
        raw_bar();
        if (!gemm) {
            if (bid == 255 && w == 0) {
                unsigned spins = 0;
                for (;;) {
                    bool ok = true;
#pragma unroll
                    for (int k = 0; k < 4; ++k) { u32x2 v; ld8_sc1_wait(v, ws + WS_FS + (k * 64 + lane) * 8); ok = ok && v[0] == tag[0] && v[1] == tag[1]; }
                    if (__all(ok) || ++spins > (1u << 18)) break;
                    __builtin_amdgcn_s_sleep(8);
                }
                if (lane == 0) st8_wt(ws + WS_E, tag);
            }
            return;
        }
        const int t32 = lane & 31, hh = lane >> 5, q4 = (lane & 15) >> 2, p4 = lane & 3;
        issue2w(ic<6>{});
        wait_vm_pin<ld_count(P_CHK1 + 18, G_FH + 1)>(fv); { poll_flags(fv, hflag, lane < 24, tag); }
        static_for<0, P2_SL>(issue2h);
        ld8_sc1(fv, hflag);
        f32x16 acc[2][2];
#pragma unroll
        for (int a = 0; a < 2; ++a)
#pragma unroll
            for (int b = 0; b < 2; ++b)
#pragma unroll
                for (int r = 0; r < 16; ++r) acc[a][b][r] = 0.f;
        unsigned rbase[3][4];
#pragma unroll
        for (int z = 0; z < 3; ++z) {
#pragma unroll
            for (int ct = 0; ct < 2; ++ct) rbase[z][ct] = (unsigned)opaque(z * 65536 + w * 4096 + 2048 + (8 * hh + q4) * 128 + (((2 * (ct ^ (w & 1)) + (g & 1)) ^ (q4 & 2)) << 5) + p4 * 8);
            rbase[z][2] = (unsigned)opaque(z * 65536 + w * 4096 + (w >> 1) * 1024 + lane * 16); rbase[z][3] = (unsigned)opaque(z * 65536 + w * 4096 + (1 - (w >> 1)) * 1024 + lane * 16);
        }
        bf16x8 wfr[2][2], hfr[2][2];
        auto rd_stage = [&](auto I) {
            constexpr int i = I; constexpr int so = p2_slot(i) * 16384, b = i & 1, z = so >> 16, im = so & 65535;
#pragma unroll
            for (int ct = 0; ct < 2; ++ct) wfr[b][ct] = cat8(tr_read(lds + rbase[z][ct] + im), tr_read(lds + rbase[z][ct] + im + 512));
            hfr[b][0] = *(const LAS bf16x8*)(lds + rbase[z][2] + im); hfr[b][1] = *(const LAS bf16x8*)(lds + rbase[z][3] + im);
        };
        f32x16 gfin;
        auto hand_over = [&](int base) {
            LAS f32x4* scr = (LAS f32x4*)(lds + base);
#pragma unroll
            for (int d = 1; d < 4; ++d) {
                const f32x16& a = acc[d & 1][d >> 1];
#pragma unroll
                for (int k = 0; k < 4; ++k) scr[(d - 1) * 1024 + w * 256 + k * 64 + lane] = f32x4{a[4 * k], a[4 * k + 1], a[4 * k + 2], a[4 * k + 3]};
            }
        };
        auto collect = [&](int base, f32x16& res) {
            const LAS f32x4* scr = (const LAS f32x4*)(lds + base);
#pragma unroll
            for (int d = 1; d < 4; ++d)
#pragma unroll
                for (int k = 0; k < 4; ++k) { const f32x4 v = scr[(d - 1) * 1024 + (w ^ d) * 256 + k * 64 + lane]; res[4 * k] += v[0]; res[4 * k + 1] += v[1]; res[4 * k + 2] += v[2]; res[4 * k + 3] += v[3]; }
        };
        const int tok = ti * 64 + (w >> 1) * 32 + t32, col0 = cj * 64 + (w & 1) * 32 + 8 * hh;
        u32x4 xa, xb;
        wait_vm<ld_count(P_STEP - 1, G_S2 + 0)>();
        rd_stage(ic<0>{});
        static_for<0, P2_NS>([&](auto I) {
            constexpr int i = I; constexpr int b = i & 1;
            asm volatile("s_waitcnt vmcnt(%0) lgkmcnt(0)" ::"n"(i == P2_ND - 1 ? 63 : p2_wait(i)) : "memory");
            if constexpr (i == 5) { wait_vm_pin<ld_count(P_CHK2 + 5, G_FH + 2)>(fv); poll_flags(fv, hflag, lane < 40, tag); }
            if constexpr (i == 13) { wait_vm_pin<ld_count(P_CHK2 + 13, G_FH + 3)>(fv); poll_flags(fv, hflag, lane < 56, tag); }
            if constexpr (i + P2_SL < P2_NS) issue2(ic<(i + P2_SL < P2_NS ? i + P2_SL : 0)>{});
            if constexpr (i == 7) ld8_sc1(fv, hflag);
            if constexpr (i == P2_POLLO) ld8_sc1(fv, oflag);
            if constexpr (i == P2_OLD) {
                wait_vm_pin<ld_count(P_CHK2 + 100 + P2_OLD, G_FO)>(fv);
                { poll_flags(fv, oflag, lane < 32, tag); }
                raw_bar();
                static_for<P2_ND, P2_ND + P2_OE>(issue2o);
            }
            if constexpr (i + 1 < P2_NS && i != P2_ND - 1) rd_stage(ic<(i + 1 < P2_NS ? i + 1 : 0)>{});
#pragma unroll
            for (int ct = 0; ct < 2; ++ct)
#pragma unroll
                for (int tt = 0; tt < 2; ++tt) acc[ct][tt] = MFMA32(wfr[b][ct], hfr[b][tt], acc[ct][tt]);
            if constexpr (i == P2_ND - 1) {
                hand_over(0); gfin = acc[0][0];
                lds_bar();
                collect(0, gfin);
#pragma unroll
                for (int j = 0; j < 16; ++j) { const float f = rbf(gfin[j]); gfin[j] = rbf(f * __builtin_amdgcn_rcpf(1.0f + __expf(-f))); }
                wait_vm<p2_wait(P2_ND - 1) + p2_ops(P2_ND - 1 + P2_SL)>();
                rd_stage(ic<P2_ND>{});
#pragma unroll
                for (int a = 0; a < 2; ++a)
#pragma unroll
                    for (int c = 0; c < 2; ++c)
#pragma unroll
                        for (int r = 0; r < 16; ++r) acc[a][c][r] = 0.f;
            }
            if constexpr (i == P2_ND + 2) ld2_plain(xa, xb, (const char*)x + (size_t)tok * 896 + col0 * 2);
        });
        hand_over(3 * 16384);
        f32x16 sfin = acc[0][0];
        asm volatile("s_waitcnt vmcnt(0) lgkmcnt(0)\n\ts_barrier" : "+v"(xa), "+v"(xb)::"memory");
        collect(3 * 16384, sfin);
        unsigned sp[8];
#pragma unroll
        for (int j = 0; j < 8; ++j) {
            float r2[2];
#pragma unroll
            for (int e = 0; e < 2; ++e) r2[e] = rbf(sfin[2 * j + e]) * gfin[2 * j + e];
            sp[j] = pk_bf16(r2[0], r2[1]);
        }
#pragma unroll
        for (int n = 0; n < 2; ++n) {
            const auto r0 = __builtin_amdgcn_permlane32_swap(sp[4 * n], sp[4 * n + 2], false, false), r1 = __builtin_amdgcn_permlane32_swap(sp[4 * n + 1], sp[4 * n + 3], false, false);
            const u32x4 sg = {r0[0], r1[0], r0[1], r1[1]}, xx = n ? xb : xa;
            u32x4 o;
#pragma unroll
            for (int j = 0; j < 4; ++j) o[j] = pk_bf16(lo_f(xx[j]) + lo_f(sg[j]), hi_f(xx[j]) + hi_f(sg[j]));
            *(GAS u32x4*)((GAS char*)out + (size_t)tok * 896 + col0 * 2 + n * 32) = o;
        }
        return;
    }

    const int wm = wid >> 2, wn = wid & 3;
    const int t32 = lane & 31, hh = lane >> 5;
    u32x4 xv[7]; u32x2 ev;
    {
        const char* xp = (const char*)x + (size_t)(rt * 64 + (tid >> 3)) * 896 + (tid & 7) * 16;
        asm volatile("global_load_dwordx4 %0, %7, off\n\tglobal_load_dwordx4 %1, %7, off offset:128\n\tglobal_load_dwordx4 %2, %7, off offset:256\n\tglobal_load_dwordx4 %3, %7, off offset:384\n\t"
                     "global_load_dwordx4 %4, %7, off offset:512\n\tglobal_load_dwordx4 %5, %7, off offset:640\n\tglobal_load_dwordx4 %6, %7, off offset:768"
                     : "=&v"(xv[0]), "=&v"(xv[1]), "=&v"(xv[2]), "=&v"(xv[3]), "=&v"(xv[4]), "=&v"(xv[5]), "=&v"(xv[6]) : "v"(xp) : "memory");
    }
    ld8_sc1(ev, ws + WS_E);
    asm volatile("s_waitcnt vmcnt(1)" : "+v"(xv[0]), "+v"(xv[1]), "+v"(xv[2]), "+v"(xv[3]), "+v"(xv[4]), "+v"(xv[5]), "+v"(xv[6])::"memory");
    {
        float v[56]; float s0 = 0.f, s1 = 0.f;
#pragma unroll
        for (int i = 0; i < 7; ++i)
#pragma unroll
            for (int j = 0; j < 4; ++j) { v[i * 8 + 2 * j] = lo_f(xv[i][j]); v[i * 8 + 2 * j + 1] = hi_f(xv[i][j]); s0 += v[i * 8 + 2 * j]; s1 += v[i * 8 + 2 * j + 1]; }
        const float mean = sum8(s0 + s1) * (1.0f / 448.f), mb = rbf(mean);
        float q0 = 0.f, q1 = 0.f;
#pragma unroll
        for (int i = 0; i < 28; ++i) { v[2 * i] -= mb; v[2 * i + 1] -= mb; q0 = __builtin_fmaf(v[2 * i], v[2 * i], q0); q1 = __builtin_fmaf(v[2 * i + 1], v[2 * i + 1], q1); }
        const float dm = mean - mb, var = __builtin_fmaf(-dm, dm, sum8(q0 + q1) * (1.0f / 448.f));
        const float den = rbf(__builtin_sqrtf(rbf(rbf(var) + 1e-5f))), rden = 1.0f / den;
        const int row = tid >> 3, c8 = tid & 7;
        LAS char* dst = lds + (((row >> 5) * 28 + (c8 >> 1)) * 64 + (c8 & 1) * 32 + ((row & 31) ^ c8)) * 16;
#pragma unroll
        for (int i = 0; i < 7; ++i) {
            u32x4 o;
#pragma unroll
            for (int j = 0; j < 4; ++j) { const unsigned dp = pk_bf16(v[i * 8 + 2 * j], v[i * 8 + 2 * j + 1]); o[j] = pk_bf16(lo_f(dp) * rden, hi_f(dp) * rden); }
            *(LAS u32x4*)(dst + i * 4096) = o;
        }
    }
    asm volatile("s_waitcnt vmcnt(0) lgkmcnt(0)\n\ts_barrier" : "+v"(ev)::"memory");
    const unsigned tl_ = __builtin_amdgcn_readfirstlane(ev[0]) + 1u, th_ = __builtin_amdgcn_readfirstlane(ev[1]) + (tl_ == 0u ? 1u : 0u);
    const u32x2 tag = {tl_, th_};
    bf16x8 xf[28];
#pragma unroll
    for (int s = 0; s < 28; ++s) xf[s] = *(const LAS bf16x8*)(lds + ((wm * 28 + s) * 64 + hh * 32 + (t32 ^ ((2 * s + hh) & 7))) * 16);
    lds_bar();
    {
        f32x16 accs[2];
        unsigned rdo, rds = 0;
        auto set_rd = [&](int u) {
            const int ln = opaque(lane), hh = ln >> 5, q4 = (ln & 15) >> 2, p4 = ln & 3, chh = (ln >> 4) & 1;
            const int kind = unit_kind(u);
            int rg, pos;
            if (kind == 3) { rg = chh; pos = wn * 32 + 8 * p4; }
            else { const int b = 56 * wn + 32 * chh + 8 * ((chh && p4 == 3) ? 2 : p4), o = 224 * unit_idx(u), a = (o & 127) + b;
                   rg = (a >> 7) == 1; pos = a < 128 ? b : a < 256 ? a - 128 : (128 - (o & 127)) + (a - 256); }
            const int sw = (kind == 3) ? (((q4 >> 1) & 1) | (rg << 1)) : (q4 & 2);
            rdo = (unsigned)(rg * 8192 + (8 * hh + q4) * 128 + ((((pos >> 5) ^ sw) << 5) | (pos & 31)));
        };
        bf16x8 wf[3];
        auto rd_frag = [&](auto FF) {
            constexpr int Fi = FF; constexpr int i = Fi / 4, kq = Fi % 4, slot = (i + 4) % 10;
            if constexpr (Fi % 28 == 0) set_rd(i / 7);
            if constexpr (kq == 0) rds = (unsigned)opaque((int)(rdo + slot * 16384));
            const LAS char* b = lds + rds + kq * 2048;
            wf[Fi % 3] = cat8(tr_read(b), tr_read(b + 512));
        };
        auto flag_unit = [&](int u) {
            const int kind = unit_kind(u), ui = unit_idx(u);
            const int lane = opaque(tid) & 63;
            char* f = (kind == 3) ? ws + WS_FH + ((size_t)((rt * 4 + wn) * 32 + ui) * 2 + wm) * 8 : ws + WS_FQ + ((size_t)(((4 * ui + wn) * 3 + kind) * 32 + rt) * 2 + wm) * 8;
            if (lane == 0) st8_wt(f, tag);
        };
        auto epilogue = [&](auto U) {
            constexpr int u = U;
            const f32x16& acc = accs[u & 1];
            const int kind = (u <= 1) ? unit_kind(u) : 3, ui = unit_idx(u);
            const int ln = opaque(tid) & 63, t32 = ln & 31, hh = ln >> 5;
            if (kind == 3) {
                unsigned hp[4];
#pragma unroll
                for (int j = 0; j < 4; ++j) {
                    const unsigned gp = pk_bf16(acc[2 * j], acc[2 * j + 1]), up = pk_bf16(acc[8 + 2 * j], acc[8 + 2 * j + 1]);
                    hp[j] = pk_bf16(lo_f(gp) * lo_f(up), hi_f(gp) * hi_f(up));
                }
                const auto r0 = __builtin_amdgcn_permlane32_swap(hp[0], hp[2], false, false), r1 = __builtin_amdgcn_permlane32_swap(hp[1], hp[3], false, false);
                st16_wt(ws + WS_H + ((size_t)((rt * 2 + wm) * 224 + ui * 8 + wn * 2 + hh) * 32 + t32) * 16, u32x4{r0[0], r1[0], r0[1], r1[1]});
            } else {
                unsigned p[8];
#pragma unroll
                for (int j = 0; j < 8; ++j) p[j] = pk_bf16(acc[2 * j], acc[2 * j + 1]);
                if (hh) { p[6] = (kind == 2) ? 0x00003f80u : 0u; p[7] = 0u; }
                char* base = ws + (kind == 0 ? WS_Q : kind == 1 ? WS_K : WS_V) + (size_t)((4 * ui + wn) * 128 + rt * 4 + wm * 2 + (t32 >> 4)) * 1024;
                const int tt = (kind == 2) ? ((t32 & 15) ^ (8 * hh)) : (t32 & 15);
#pragma unroll
                for (int n = 0; n < 2; ++n) {
                    const auto r0 = __builtin_amdgcn_permlane32_swap(p[4 * n], p[4 * n + 2], false, false), r1 = __builtin_amdgcn_permlane32_swap(p[4 * n + 1], p[4 * n + 3], false, false);
                    st16_wt(base + ((2 * n + hh) * 16 + tt) * 16, u32x4{r0[0], r1[0], r0[1], r1[1]});
                }
            }
        };
        const int head = xcd * 2 + (idx >> 4), qt = idx & 15, a = wid;
        u32x2 fq; u32x4 qv;
        const int tq = qt * 8 + a;
        auto qflag = [&]() -> const char* { int z = opaque(0); return ws + WS_FQ + ((size_t)((head * 3) * 32 + (tq >> 2)) * 2 + ((tq >> 1) & 1)) * 8 + z; };
        auto qsrc = [&]() -> const char* { return ws + WS_Q + (size_t)(head * 128 + tq) * 1024 + (opaque(tid) & 63) * 16; };
        raw_bar();
        rd_frag(ic<0>{}); rd_frag(ic<1>{});
        static_for<0, 4 * P1_NSUB>([&](auto FF) {
            constexpr int Fi = FF;
            if constexpr (Fi % 8 == 0 && Fi > 0) { raw_bar(); }
            if constexpr (Fi + 2 < 4 * P1_NSUB) rd_frag(ic<(Fi + 2 < 4 * P1_NSUB ? Fi + 2 : 0)>{});
            constexpr int u = Fi / 28;
            if constexpr (Fi % 28 == 0) {
#pragma unroll
                for (int r = 0; r < 16; ++r) accs[u & 1][r] = 0.f;
            }
            accs[u & 1] = MFMA32(wf[Fi % 3], xf[Fi % 28], accs[u & 1]);
            if constexpr (Fi % 28 == 2 && u >= 1) epilogue(ic<(u >= 1 ? u - 1 : 0)>{});
            if constexpr (Fi == 4 * P1_NSUB - 1) epilogue(ic<4>{});
            if constexpr (Fi % 28 == 10 && u >= 1) { wait_vm<0>(); flag_unit(u - 1); }
            if constexpr (Fi == 112 + 15) ld8_sc1(fq, qflag());
            if constexpr (Fi == 112 + 24) { wait_vm_pin<0>(fq); poll_flags(fq, qflag(), true, tag); ld16_sc1(qv, qsrc()); }
        });
        asm volatile("s_waitcnt vmcnt(%1) lgkmcnt(0)\n\ts_barrier" : "+v"(qv) : "n"(1) : "memory");
        const int lane = opaque(tid) & 63, g = lane >> 4, q4 = (lane & 15) >> 2, p4 = lane & 3;
        constexpr unsigned LK = 0, LV = 24576;
        const int t16 = lane & 15;
        const bf16x8 qf = __builtin_bit_cast(bf16x8, qv);
        f32x4 sc[17];
#pragma unroll
        for (int j = 0; j < 17; ++j) { const bf16x8 kf = *(const LAS bf16x8*)(lds + LK + (a + j) * 1024 + lane * 16); sc[j] = MFMA16(kf, qf, (f32x4{0.f, 0.f, 0.f, 0.f})); }
        SBAR(); asm volatile("s_nop 15"); SBAR();
        const float NEG = -1e30f, L2E = 1.4426950408889634f, SCALE = 0.18898223650461362f;
#pragma unroll
        for (int r = 0; r < 4; ++r) { if (4 * g + r <= t16) sc[0][r] = NEG; if (4 * g + r > t16) sc[16][r] = NEG; }
        if (qt < 2) {
            asm volatile("" ::: "memory");
            const int first = 16 - qt * 8 - a;
#pragma unroll
            for (int j = 0; j < 16; ++j) if (j < first) sc[j] = f32x4{NEG, NEG, NEG, NEG};
        }
        float mx = max3f(sc[0][0], sc[0][1], sc[0][2]);
        mx = max3f(mx, sc[0][3], sc[1][0]);
        mx = max3f(mx, sc[1][1], sc[1][2]);
#pragma unroll
        for (int j = 2; j < 16; ++j) { mx = max3f(mx, sc[j][0], sc[j][1]); mx = max3f(mx, sc[j][2], sc[j][3]); }
        mx = max3f(mx, sc[16][0], sc[16][1]); mx = max3f(mx, sc[16][2], sc[16][3]);
        mx = max2f_pad(mx, sc[1][3]);
        { const auto r = __builtin_amdgcn_permlane16_swap(__float_as_uint(mx), __float_as_uint(mx), false, false); mx = max2f_pad(__uint_as_float(r[0]), __uint_as_float(r[1])); }
        { const auto r = __builtin_amdgcn_permlane32_swap(__float_as_uint(mx), __float_as_uint(mx), false, false); mx = max2f_pad(__uint_as_float(r[0]), __uint_as_float(r[1])); }
        wait_vm<0>(); flag_unit(4);
        const float nm = -rbf(rbf(mx) * SCALE) * L2E;
        const f32x2 nm2 = {nm, nm}, l2 = {L2E, L2E}, sc2 = {SCALE, SCALE};
        u32x2 pp[18];
#pragma unroll
        for (int j = 0; j < 17; ++j) {
            unsigned pk[2];
#pragma unroll
            for (int r = 0; r < 2; ++r) {
                const unsigned p1 = pk_bf16(sc[j][2 * r], sc[j][2 * r + 1]);
                const f32x2 s1 = f32x2{lo_f(p1), hi_f(p1)} * sc2;
                const unsigned p2 = pk_bf16(s1[0], s1[1]);
                const f32x2 e = __builtin_elementwise_fma(f32x2{lo_f(p2), hi_f(p2)}, l2, nm2);
                pk[r] = pk_bf16(__builtin_amdgcn_exp2f(e[0]), __builtin_amdgcn_exp2f(e[1]));
            }
            pp[j] = u32x2{pk[0], pk[1]};
        }
        pp[17] = u32x2{0u, 0u};
        f32x4 o0 = {0.f, 0.f, 0.f, 0.f}, o1 = {0.f, 0.f, 0.f, 0.f};
        const int vkey = 4 * g + q4;
        const unsigned va0 = LV + a * 1024 + (((p4 >> 1) * 16 + (vkey ^ (8 * (p4 >> 1)))) * 16) + (p4 & 1) * 8;
#pragma unroll
        for (int jp = 0; jp < 9; ++jp) {
            const int ja = 2 * jp, jb = (jp < 8) ? 2 * jp + 1 : 2 * jp;
            const bf16x8 pf = __builtin_bit_cast(bf16x8, (u32x4{pp[2 * jp][0], pp[2 * jp][1], pp[2 * jp + 1][0], pp[2 * jp + 1][1]}));
            const bf16x8 v0 = cat8(tr_read(lds + va0 + ja * 1024), tr_read(lds + va0 + jb * 1024));
            const bf16x8 v1 = cat8(tr_read(lds + va0 + ja * 1024 + 512), tr_read(lds + va0 + jb * 1024 + 512));
            o0 = MFMA16(v0, pf, o0); o1 = MFMA16(v1, pf, o1);
        }
        const float l = __shfl(o1[0], 48 + t16), rl = 1.0f / l;
        u32x4 ov = {pk_bf16(o0[0] * rl, o0[1] * rl), pk_bf16(o0[2] * rl, o0[3] * rl), pk_bf16(o1[0] * rl, o1[1] * rl), pk_bf16(o1[2] * rl, o1[3] * rl)};
        if (g == 3) { ov[2] = 0u; ov[3] = 0u; }
        const int tl = qt * 8 + a;
        st16_wt(ws + WS_O + ((size_t)((tl >> 1) * 16 + head) * 4 + g) * 512 + ((tl & 1) * 16 + t16) * 16, ov);
        wait_vm<0>();
        if (lane == 0) st8_wt(ws + WS_FO + ((size_t)(((tl >> 2) * 2 + (head & 1)) * 8 + (head >> 1)) * 4 + (tl & 3)) * 8, tag);
    }
}

extern "C" void kernel_launch(void* const* d_in, const int* in_sizes, int n_in, void* d_out, int out_size, void* d_ws, size_t ws_size, hipStream_t stream) {
    const u16* x = (const u16*)d_in[0]; const u16* wqkv = (const u16*)d_in[1]; const u16* wo = (const u16*)d_in[2];
    const u16* wg = (const u16*)d_in[4]; const u16* wu = (const u16*)d_in[5]; const u16* wd = (const u16*)d_in[6];
    fused_kernel<<<256, 768, 0, stream>>>(x, wqkv, wo, wg, wu, wd, (char*)d_ws, (u16*)d_out);
}
```
